```python
import torch

def _softmax(x, axis=-1):
    return torch.softmax(x, dim=axis)
def _gelu(x):
    return torch.nn.functional.gelu(x, approximate="tanh")
def _silu(x):
    return torch.nn.functional.silu(x)
def _sigmoid(x):
    return torch.sigmoid(x)
def _layernorm(x, axis=-1, eps=1e-5):
    m = x.mean(dim=axis, keepdim=True)
    v = x.var(dim=axis, keepdim=True, unbiased=False)
    return (x - m) / torch.sqrt(v + eps)
def _scatter_add(tgt, idx, src, axis=0):
    return tgt.clone().index_add_(axis, idx, src)
def _segment_sum(src, seg, n):
    out = torch.zeros((n,) + src.shape[1:], dtype=src.dtype)
    return out.index_add_(0, seg, src)
def _pad(x, pw):
    flat = tuple(v for pair in reversed(pw) for v in pair)
    return torch.nn.functional.pad(x, flat)

kernel_name = 'recurrent_scan__gqa_block_cached__mixer_channel_mlp__decode'

def setup_inputs():

    _w = (lambda a: (lambda t: t.to(torch.bfloat16) if t.is_floating_point() else t)(torch.as_tensor(a)))
    def _A_setup():
        B, Sq, D = 4, 1, 512
        Hq, Hkv, Sk, Dh = 32, 8, 16384, 128
        assert Hq % Hkv == 0, (Hq, Hkv)
        return {
            "x": _w((lambda *s: torch.randn(*s, dtype=torch.bfloat16) * 0.1)(B, Sq, D)),
            "k_cache": _w((lambda *s: torch.randn(*s, dtype=torch.bfloat16) * 0.8)(B, Hkv, Sk, Dh)),
            "v_cache": _w((lambda *s: torch.randn(*s, dtype=torch.bfloat16) * 3.2)(B, Hkv, Sk, Dh)),
            "wq": _w((lambda *s: torch.randn(*s, dtype=torch.bfloat16) * 0.8)(D, Hq * Dh) * (D ** -0.5)),
            "wo": _w((lambda *s: torch.randn(*s, dtype=torch.bfloat16) * 3.2)(Hq * Dh, D) * ((Hq * Dh) ** -0.5)),
        }
    _A_in = _A_setup()

    def _B_setup():
        B, S, D, F = 4, 1, 512, 1792
        return {
            "x": _w((lambda *s: torch.randn(*s, dtype=torch.bfloat16) * 0.1)(B, S, D)),
            "w1": _w((lambda *s: torch.randn(*s, dtype=torch.bfloat16) * 3.2)(D, F) * (D ** -0.5)),
            "b1": _w((lambda *s: torch.randn(*s, dtype=torch.bfloat16) * 3.2)(F)),
            "w2": _w((lambda *s: torch.randn(*s, dtype=torch.bfloat16) * 3.2)(F, D) * (F ** -0.5)),
            "b2": _w((lambda *s: torch.randn(*s, dtype=torch.bfloat16) * 3.2)(D)),
        }
    _B_in = _B_setup()

    return {
        "A_x": _A_in["x"],
        "A_k_cache": _A_in["k_cache"],
        "A_v_cache": _A_in["v_cache"],
        "A_wq": _A_in["wq"],
        "A_wo": _A_in["wo"],
        "B_x": _B_in["x"],
        "B_w1": _B_in["w1"],
        "B_b1": _B_in["b1"],
        "B_w2": _B_in["w2"],
        "B_b2": _B_in["b2"],
    }

def reference(A_x, A_k_cache, A_v_cache, A_wq, A_wo, B_x, B_w1, B_b1, B_w2, B_b2):
    def _A_ref(x, k_cache, v_cache, wq, wo):
        B, Sq, D = x.shape
        Hq, Dh = 32, 128
        Hkv = k_cache.shape[1]
        rep = Hq // Hkv
        q = (lambda x, *d: x.permute(*d))((x @ wq).reshape(B, Sq, Hq, Dh), 0, 2, 1, 3)
        k = (lambda x, n, axis: torch.repeat_interleave(x, n, dim=axis))(k_cache, rep, axis=1)
        v = (lambda x, n, axis: torch.repeat_interleave(x, n, dim=axis))(v_cache, rep, axis=1)
        scores = torch.einsum("bhqd,bhkd->bhqk", q, k) * (Dh ** -0.5)
        p = _softmax(scores, axis=-1)
        o = torch.einsum("bhqk,bhkd->bhqd", p, v)
        return (lambda x, *d: x.permute(*d))(o, 0, 2, 1, 3).reshape(B, Sq, Hq * Dh) @ wo

    def _B_ref(x, w1, b1, w2, b2):
        y = _layernorm(x, axis=-1)
        h = _gelu(torch.einsum("bsd,df->bsf", y, w1) + b1)
        return torch.einsum("bsf,fd->bsd", h, w2) + b2

    x = A_x
    xn = _layernorm(x, axis=-1)
    s = _A_ref(xn, A_k_cache, A_v_cache, A_wq, A_wo)
    g = _silu(_B_ref(xn, B_w1, B_b1, B_w2, B_b2))
    return x + s * g
```

```cpp
#include <hip/hip_runtime.h>
#include <hip/hip_bf16.h>
#include <stdint.h>

namespace {

typedef unsigned v4u __attribute__((ext_vector_type(4)));
typedef unsigned v2u __attribute__((ext_vector_type(2)));
typedef float f32x4 __attribute__((ext_vector_type(4)));
typedef short bf16x8 __attribute__((ext_vector_type(8)));
typedef unsigned long long u64;
typedef unsigned short u16;
#define GAS __attribute__((address_space(1)))
#define RLX_AGENT __ATOMIC_RELAXED, __HIP_MEMORY_SCOPE_AGENT

constexpr int DM = 512, SK = 16384, FF = 1792;
constexpr int NWG = 256, NTHR = 512;
constexpr int W = 32;
constexpr int W1 = 16;
constexpr int RSRC_W3 = 0x00020000;
constexpr int AUX_NT = 2;
constexpr unsigned SPIN_CAP = 1u << 16;

constexpr size_t G_EPOCH = 0;
constexpr size_t G_QX = 512;
constexpr size_t G_HX = G_QX + 4 * 4096;
constexpr size_t G_PX = G_HX + 4 * 2048;
constexpr size_t G_PS = G_PX + 32 * 8 * 512;
constexpr size_t G_SX = G_PS + 32 * 8 * 16;
constexpr size_t G_END = G_SX + 4 * 64 * 64 * 8;

constexpr int L_XS = 0, L_YS = 8192, L_RED = 16384, L_REDH = 18432, L_QS = 20480, L_OM = 24576, L_SM = 40960,
              L_SL = L_SM + 128, L_CV = 41472, L_CM = L_CV + 2048, L_CL = L_CM + 32, L_M2 = L_CM + 64,
              L_R3 = L_M2 + 256, L_END = L_R3 + 256;
constexpr int LDS_BYTES = 96 * 1024;
static_assert(L_END <= LDS_BYTES, "lds");

struct Params {
  const u16* x; const u16* k; const u16* v; const u16* wq; const u16* wo;
  const u16* w1; const u16* b1; const u16* w2; const u16* b2;
  u16* out; u64* ws;
};

__device__ __forceinline__ float asf(unsigned u) { return __builtin_bit_cast(float, u); }
__device__ __forceinline__ unsigned asu(float f) { return __builtin_bit_cast(unsigned, f); }
__device__ __forceinline__ float bflo(unsigned u) { return asf(u << 16); }
__device__ __forceinline__ float bfhi(unsigned u) { return asf(u & 0xffff0000u); }
__device__ __forceinline__ unsigned f2bf(float f) { return (unsigned)__builtin_bit_cast(u16, (__bf16)f); }
__device__ __forceinline__ float rbf(float f) { return (float)(__bf16)f; }
__device__ __forceinline__ unsigned cvtpk(float lo, float hi) {
  typedef __bf16 bf2 __attribute__((ext_vector_type(2)));
  bf2 r; r.x = (__bf16)lo; r.y = (__bf16)hi;
  return __builtin_bit_cast(unsigned, r);
}
template <int CTRL> __device__ __forceinline__ float dpp(float x) {
  return __builtin_bit_cast(float, __builtin_amdgcn_mov_dpp(__builtin_bit_cast(int, x), CTRL, 0xf, 0xf, true));
}
constexpr int XOR1 = 0xB1, XOR2 = 0x4E, HMIR = 0x141, MIR = 0x140, ROR8 = 0x128;
__device__ __forceinline__ float tr32(float a, float b) {
  auto r = __builtin_amdgcn_permlane32_swap(asu(a), asu(b), false, false);
  return asf(r[0]) + asf(r[1]);
}
__device__ __forceinline__ float tr16(float a, float b) {
  auto r = __builtin_amdgcn_permlane16_swap(asu(a), asu(b), false, false);
  return asf(r[0]) + asf(r[1]);
}
__device__ __forceinline__ float max_rows(float x) {
  auto s = __builtin_amdgcn_permlane16_swap(asu(x), asu(x), false, false);
  x = fmaxf(asf(s[0]), asf(s[1]));
  auto t = __builtin_amdgcn_permlane32_swap(asu(x), asu(x), false, false);
  return fmaxf(asf(t[0]), asf(t[1]));
}
__device__ __forceinline__ float sum_rows(float x) { x = tr16(x, x); return tr32(x, x); }
__device__ __forceinline__ float wave_sum(float x) {
  x += dpp<XOR1>(x); x += dpp<XOR2>(x); x += dpp<HMIR>(x); x += dpp<MIR>(x);
  return sum_rows(x);
}
__device__ __forceinline__ float treduce8(const float (&v)[8], int lane) {
  const float u0 = tr32(v[0], v[4]), u1 = tr32(v[1], v[5]), u2 = tr32(v[2], v[6]), u3 = tr32(v[3], v[7]);
  const float w0 = tr16(u0, u2), w1 = tr16(u1, u3);
  const float t0 = w0 + dpp<ROR8>(w0), t1 = w1 + dpp<ROR8>(w1);
  return (lane & 8) ? t1 : t0;
}
__device__ __forceinline__ void ln_row(const float (&v)[8], float (&o)[8]) {
  float s = 0.f;
#pragma unroll
  for (int i = 0; i < 8; ++i) s += v[i];
  s = wave_sum(s);
  const float mean = s * (1.0f / DM);
  float ss = 0.f;
#pragma unroll
  for (int i = 0; i < 8; ++i) { const float d = v[i] - mean; ss = fmaf(d, d, ss); }
  ss = wave_sum(ss);
  const float var_b = rbf(ss * (1.0f / DM)), m_b = rbf(mean);
  const float den = rbf(sqrtf(rbf(var_b + 1e-5f)));
#pragma unroll
  for (int i = 0; i < 8; ++i) o[i] = rbf(rbf(v[i] - m_b) / den);
}
#define WG_BARRIER() asm volatile("s_waitcnt lgkmcnt(0)\n\ts_barrier" ::: "memory")
#define SCHED() __builtin_amdgcn_sched_barrier(0)

struct Tag { unsigned lo8, hi; };
__device__ __forceinline__ void gstore(u64* p, unsigned payload24, Tag t) {
  __hip_atomic_store((GAS u64*)p, ((u64)t.hi << 32) | (u64)(payload24 | t.lo8), RLX_AGENT);
}
__device__ __forceinline__ u64 gload(const u64* p) { return __hip_atomic_load((const GAS u64*)p, RLX_AGENT); }
__device__ __forceinline__ bool gok(u64 g, Tag t) {
  return (unsigned)(g >> 32) == t.hi && ((unsigned)g & 0xff000000u) == t.lo8;
}
__device__ __forceinline__ unsigned enc24(float f) { unsigned u = asu(f); u += 0x7fu + ((u >> 8) & 1u); return u >> 8; }
__device__ __forceinline__ float dec24(u64 g) { return asf((unsigned)g << 8); }

__device__ __forceinline__ f32x4 mfma(v4u a, v4u b, f32x4 c) {
  return __builtin_amdgcn_mfma_f32_16x16x32_bf16(__builtin_bit_cast(bf16x8, a), __builtin_bit_cast(bf16x8, b), c, 0, 0, 0);
}

__device__ __forceinline__ float gemv16(const unsigned (&wv)[8], const float* vec, int rg, int lane) {
  float acc[8];
#pragma unroll
  for (int bb = 0; bb < 4; ++bb) {
    const f32x4 x0 = *(const f32x4*)(vec + bb * DM + 8 * rg), x1 = *(const f32x4*)(vec + bb * DM + 8 * rg + 4);
    float a0 = 0.f, a1 = 0.f;
#pragma unroll
    for (int r = 0; r < 4; ++r) { a0 = fmaf(x0[r], bflo(wv[r]), a0); a1 = fmaf(x0[r], bfhi(wv[r]), a1); }
#pragma unroll
    for (int r = 0; r < 4; ++r) { a0 = fmaf(x1[r], bflo(wv[4 + r]), a0); a1 = fmaf(x1[r], bfhi(wv[4 + r]), a1); }
    acc[bb * 2] = a0; acc[bb * 2 + 1] = a1;
  }
  return treduce8(acc, lane);
}

__global__ void __launch_bounds__(NTHR, 2) fused_decode(Params p) {
  __shared__ __attribute__((aligned(16))) unsigned char lds[LDS_BYTES];
  const int tid = threadIdx.x, lane = tid & 63, w = __builtin_amdgcn_readfirstlane(tid >> 6);
  const int bid = blockIdx.x, g = bid & 7, j = bid >> 3, b = j >> 3, s = j & 7, pair = b * 8 + g, cb = g * 8 + s;
  u64* ws = p.ws;

  const u64 e_raw = gload(ws + G_EPOCH);
  const v4u xr = *(const GAS v4u*)(p.x + (w & 3) * DM + 8 * lane);
  const int cp = tid & 7, rg = tid >> 3;
  unsigned wqv[8], w1v[8];
  {
    const GAS u16* q0 = (const GAS u16*)p.wq + (size_t)(8 * rg) * 4096 + g * 512 + 16 * j + 2 * cp;
#pragma unroll
    for (int r = 0; r < 8; ++r) wqv[r] = *(const GAS unsigned*)(q0 + r * 4096);
  }
  const bool hwg = j >= 18;
  const int f0 = 224 * g + 16 * (hwg ? j - 18 : 0);
  {
    const GAS u16* h0 = (const GAS u16*)p.w1 + (size_t)(8 * rg) * FF + f0 + 2 * cp;
#pragma unroll
    for (int r = 0; r < 8; ++r) w1v[r] = *(const GAS unsigned*)(h0 + r * FF);
  }
  SCHED();
  const size_t kvoff = ((size_t)pair * SK + s * 2048 + w * 32) * 128;
  const __amdgpu_buffer_rsrc_t rk = __builtin_amdgcn_make_buffer_rsrc((void*)(p.k + kvoff), 0, 7 * 65536 + 8192, RSRC_W3);
  const __amdgpu_buffer_rsrc_t rv = __builtin_amdgcn_make_buffer_rsrc((void*)(p.v + kvoff), 0, 7 * 65536 + 8192, RSRC_W3);
  const int koff = ((lane & 15) >> 2) * 256 + (lane & 3) * 16 + (lane >> 4) * 64, voff = lane * 16;
  v4u a[W];
#define ISSUE(i_) do { constexpr int I_ = (i_); if constexpr (I_ < 64) \
      a[I_ % W] = __builtin_amdgcn_raw_buffer_load_b128(rk, koff + (I_ & 3) * 1024, (I_ >> 3) * 65536 + ((I_ >> 2) & 1) * 4096, AUX_NT); \
    else if constexpr (I_ < 128) \
      a[I_ % W] = __builtin_amdgcn_raw_buffer_load_b128(rv, voff + (I_ & 3) * 1024, ((I_ - 64) >> 3) * 65536 + ((I_ >> 2) & 1) * 4096, AUX_NT); } while (0)
#define ISSUE4(i_) do { ISSUE((i_)); ISSUE((i_) + 1); ISSUE((i_) + 2); ISSUE((i_) + 3); } while (0)
#define ISSUE8(i_) do { ISSUE4((i_)); ISSUE4((i_) + 4); } while (0)
  ISSUE8(0); ISSUE8(8);
  static_assert(W1 == 16 && W == 32, "window issue code below");
  SCHED();

  float* xs = (float*)(lds + L_XS);
  float* ys = (float*)(lds + L_YS);
  {
    float xv[8], xn[8];
#pragma unroll
    for (int i = 0; i < 4; ++i) { xv[2 * i] = bflo(xr[i]); xv[2 * i + 1] = bfhi(xr[i]); }
    ln_row(xv, xn);
    float* dst = xs;
    if (w >= 4) { float y[8]; ln_row(xn, y);
#pragma unroll
      for (int i = 0; i < 8; ++i) xn[i] = y[i];
      dst = ys; }
    *(f32x4*)(dst + (w & 3) * DM + 8 * lane) = f32x4{xn[0], xn[1], xn[2], xn[3]};
    *(f32x4*)(dst + (w & 3) * DM + 8 * lane + 4) = f32x4{xn[4], xn[5], xn[6], xn[7]};
  }
  Tag tag; u64 tag40;
  {
    const u64 e = ((u64)__builtin_amdgcn_readfirstlane((unsigned)(e_raw >> 32)) << 32) | (u64)__builtin_amdgcn_readfirstlane((unsigned)e_raw);
    tag40 = ((e >> 24) * 0x5DEECE66DULL + 0xBULL) & ((1ULL << 40) - 1);
    tag.lo8 = (unsigned)(tag40 & 0xff) << 24; tag.hi = (unsigned)(tag40 >> 8);
  }
  WG_BARRIER();
  float* red = (float*)(lds + L_RED);
  float* redh = (float*)(lds + L_REDH);
  red[w * 64 + lane] = gemv16(wqv, xs, rg, lane);
  WG_BARRIER();
  const int ob = lane >> 4, ocol = 2 * (lane & 7) + ((lane >> 3) & 1);
  if (w == 0) {
    float q = 0.f;
#pragma unroll
    for (int ww = 0; ww < 8; ++ww) q += red[ww * 64 + lane];
    gstore(ws + G_QX + ob * 4096 + g * 512 + 16 * j + ocol, f2bf(q), tag);
  }
  const u64* qsrc = ws + G_QX + b * 4096 + g * 512 + tid;
  u64 gq = gload(qsrc);
  SCHED();
  ISSUE8(16); ISSUE8(24);
  SCHED();
  if (hwg) redh[w * 64 + lane] = gemv16(w1v, ys, rg, lane);
  for (unsigned spin = 0; !__all(gok(gq, tag)); ) {
    if (++spin > SPIN_CAP) break;
    __builtin_amdgcn_s_sleep(2);
    gq = gload(qsrc);
  }
  ((u16*)(lds + L_QS))[tid] = (u16)gq;
  WG_BARRIER();
  if (hwg && w == 1) {
    float hp = 0.f;
#pragma unroll
    for (int ww = 0; ww < 8; ++ww) hp += redh[ww * 64 + lane];
    const float pre = rbf(rbf(hp) + bflo(p.b1[f0 + ocol]));
    const float hv = 0.5f * pre * (1.0f + tanhf(0.7978845608028654f * (pre + 0.044715f * pre * pre * pre)));
    gstore(ws + G_HX + ob * 2048 + f0 + ocol, f2bf(hv), tag);
  }
  const v4u qf = *(const v4u*)(lds + L_QS + ((lane & 15) >> 2) * 256 + ((lane & 3) + 4 * (lane >> 4)) * 16);

  constexpr float C2 = 0.08838834764831845f * 1.4426950408889634f;
  float sc[64];
#define KSTEP(i_) do { const f32x4 d_ = mfma(a[(i_) % W], qf, f32x4{0.f, 0.f, 0.f, 0.f}); ISSUE((i_) + W); \
    sc[(i_)] = ((dpp<0x00>(d_[0]) + dpp<0x55>(d_[1])) + (dpp<0xAA>(d_[2]) + dpp<0xFF>(d_[3]))) * C2; } while (0)
#define KSTEP4(i_) do { KSTEP((i_)); KSTEP((i_) + 1); KSTEP((i_) + 2); KSTEP((i_) + 3); SCHED(); } while (0)
#define KSTEP16(i_) do { KSTEP4((i_)); KSTEP4((i_) + 4); KSTEP4((i_) + 8); KSTEP4((i_) + 12); } while (0)
  KSTEP16(0); KSTEP16(16); KSTEP16(32); KSTEP16(48);

  float m = sc[0];
#pragma unroll
  for (int i = 1; i < 64; ++i) m = fmaxf(m, sc[i]);
  m = max_rows(m);
  float lsum = 0.f;
  v4u pk[8];
#pragma unroll
  for (int t = 0; t < 8; ++t) {
#pragma unroll
    for (int k = 0; k < 4; ++k) {
      const float p0 = __builtin_amdgcn_exp2f(sc[8 * t + 2 * k] - m), p1 = __builtin_amdgcn_exp2f(sc[8 * t + 2 * k + 1] - m);
      lsum += p0 + p1;
      pk[t][k] = cvtpk(p0, p1);
    }
  }
  lsum = sum_rows(lsum);
  SCHED();

  f32x4 acc[8];
#pragma unroll
  for (int i = 0; i < 8; ++i) acc[i] = f32x4{0.f, 0.f, 0.f, 0.f};
  v4u wov[8], w2v[4];
  u64 hg[4];
  const __amdgpu_buffer_rsrc_t rwo = __builtin_amdgcn_make_buffer_rsrc((void*)(p.wo + (size_t)(g * 512 + 64 * s + 8 * w) * DM), 0, 8192, RSRC_W3);
  const __amdgpu_buffer_rsrc_t rw2 = __builtin_amdgcn_make_buffer_rsrc((void*)p.w2, 0, FF * DM * 2, RSRC_W3);
  const u64* hsrc = ws + G_HX + b * 2048 + tid;
#define VTILE(t_) do { constexpr int B_ = (64 + 8 * (t_)) % W; \
    _Pragma("unroll") for (int jj = 0; jj < 4; ++jj) { \
      v4u lo_, hi_; \
      _Pragma("unroll") for (int k2 = 0; k2 < 4; ++k2) { \
        lo_[k2] = __builtin_amdgcn_perm(a[B_ + 2 * k2 + 1][jj], a[B_ + 2 * k2][jj], 0x05040100u); \
        hi_[k2] = __builtin_amdgcn_perm(a[B_ + 2 * k2 + 1][jj], a[B_ + 2 * k2][jj], 0x07060302u); } \
      acc[2 * jj] = mfma(pk[(t_)], lo_, acc[2 * jj]); acc[2 * jj + 1] = mfma(pk[(t_)], hi_, acc[2 * jj + 1]); } \
    SCHED(); ISSUE8(64 + 8 * (t_) + W); SCHED(); } while (0)
  VTILE(0); VTILE(1); VTILE(2); VTILE(3);
  VTILE(4);
#pragma unroll
  for (int r = 0; r < 8; ++r) wov[r] = __builtin_amdgcn_raw_buffer_load_b128(rwo, voff + (r & 3) * 1024, (r >> 2) * 4096, 0);
  SCHED();
  VTILE(5);
#pragma unroll
  for (int k = 0; k < 4; ++k) w2v[k] = __builtin_amdgcn_raw_buffer_load_b128(rw2, tid * 1024 + cb * 16, k * 512 * 1024, 0);
#pragma unroll
  for (int k = 0; k < 4; ++k) hg[k] = gload(hsrc + 512 * k);
  SCHED();
  VTILE(6); VTILE(7);

  float* om = (float*)(lds + L_OM);
  float* sm = (float*)(lds + L_SM);
  float* sl = (float*)(lds + L_SL);
  *(f32x4*)(om + w * 512 + lane * 8) = f32x4{acc[0][0], acc[1][0], acc[2][0], acc[3][0]};
  *(f32x4*)(om + w * 512 + lane * 8 + 4) = f32x4{acc[4][0], acc[5][0], acc[6][0], acc[7][0]};
  if (lane < 16 && (lane & 3) == 0) { sm[w * 4 + (lane >> 2)] = m; sl[w * 4 + (lane >> 2)] = lsum; }
  WG_BARRIER();
  {
    const int h = tid >> 7;
    float M = sm[h];
#pragma unroll
    for (int ww = 1; ww < 8; ++ww) M = fmaxf(M, sm[ww * 4 + h]);
    float O = 0.f, L = 0.f;
#pragma unroll
    for (int ww = 0; ww < 8; ++ww) {
      const float f = __builtin_amdgcn_exp2f(sm[ww * 4 + h] - M);
      O = fmaf(f, om[ww * 512 + tid], O); L = fmaf(f, sl[ww * 4 + h], L);
    }
    gstore(ws + G_PX + (size_t)(pair * 8 + s) * 512 + tid, enc24(O), tag);
    const int d = tid & 127;
    if (d < 4) {
      const unsigned u = (d & 2) ? asu(L) : asu(M);
      gstore(ws + G_PS + (size_t)(pair * 8 + s) * 16 + h * 4 + d, (d & 1) ? (u & 0xffffu) : (u >> 16), tag);
    }
  }
  const u64* psrc = ws + G_PX + (size_t)(pair * 8 + w) * 512 + 64 * s + lane;
  const u64* ssrc = ws + G_PS + (size_t)(pair * 8 + w) * 16 + (s >> 1) * 4 + (lane & 3);
  u64 gp = gload(psrc), gs = gload(ssrc);
  const unsigned xo = p.x[b * DM + cb * 8 + (tid & 7)], b2o = p.b2[cb * 8 + (tid & 7)];
  SCHED();
  {
    const bool k3 = w < 4;
    for (unsigned spin = 0;;) {
      const bool okh = gok(hg[0], tag) && gok(hg[1], tag) && gok(hg[2], tag) && (!k3 || gok(hg[3], tag));
      if (__all(okh) || ++spin > SPIN_CAP) break;
      __builtin_amdgcn_s_sleep(2);
#pragma unroll
      for (int k = 0; k < 4; ++k) hg[k] = gload(hsrc + 512 * k);
    }
    float ma[8];
#pragma unroll
    for (int e = 0; e < 8; ++e) ma[e] = 0.f;
#pragma unroll
    for (int k = 0; k < 4; ++k) {
      const float hv = (k < 3 || k3) ? bflo((unsigned)hg[k]) : 0.f;
#pragma unroll
      for (int e = 0; e < 4; ++e) { ma[2 * e] = fmaf(hv, bflo(w2v[k][e]), ma[2 * e]); ma[2 * e + 1] = fmaf(hv, bfhi(w2v[k][e]), ma[2 * e + 1]); }
    }
    float r = treduce8(ma, lane);
    r += dpp<XOR1>(r); r += dpp<XOR2>(r); r += dpp<HMIR>(r);
    if ((lane & 7) == 0) ((float*)(lds + L_M2))[w * 8 + (lane >> 3)] = r;
  }
  for (unsigned spin = 0; !__all(gok(gp, tag) && gok(gs, tag)); ) {
    if (++spin > SPIN_CAP) break;
    __builtin_amdgcn_s_sleep(2);
    gp = gload(psrc); gs = gload(ssrc);
  }
  float* cv = (float*)(lds + L_CV);
  cv[w * 64 + lane] = dec24(gp);
  if (lane < 4) ((u16*)(lds + ((lane & 2) ? L_CL : L_CM)))[w * 2 + ((lane & 1) ^ 1)] = (u16)gs;
  WG_BARRIER();
  {
    const float* cm = (const float*)(lds + L_CM);
    const float* cl = (const float*)(lds + L_CL);
    float M = cm[0];
#pragma unroll
    for (int ww = 1; ww < 8; ++ww) M = fmaxf(M, cm[ww]);
    float O = 0.f, L = 0.f;
#pragma unroll
    for (int ww = 0; ww < 8; ++ww) {
      const float f = __builtin_amdgcn_exp2f(cm[ww] - M);
      O = fmaf(f, cv[ww * 64 + lane], O); L = fmaf(f, cl[ww], L);
    }
    const float o = rbf(O / L);
    float sa[8];
#pragma unroll
    for (int e = 0; e < 8; ++e) sa[e] = 0.f;
#pragma unroll
    for (int r = 0; r < 8; ++r) {
      const float ov = asf(__builtin_amdgcn_readlane(asu(o), 8 * w + r));
#pragma unroll
      for (int e = 0; e < 4; ++e) { sa[2 * e] = fmaf(ov, bflo(wov[r][e]), sa[2 * e]); sa[2 * e + 1] = fmaf(ov, bfhi(wov[r][e]), sa[2 * e + 1]); }
    }
    *(f32x4*)(om + w * 512 + lane * 8) = f32x4{sa[0], sa[1], sa[2], sa[3]};
    *(f32x4*)(om + w * 512 + lane * 8 + 4) = f32x4{sa[4], sa[5], sa[6], sa[7]};
  }
  WG_BARRIER();
  {
    float sp = 0.f;
#pragma unroll
    for (int ww = 0; ww < 8; ++ww) sp += om[ww * 512 + tid];
    gstore(ws + G_SX + ((size_t)(b * 64 + (tid >> 3)) * 64 + cb) * 8 + (tid & 7), enc24(sp), tag);
  }
  const u64* fsrc = ws + G_SX + (size_t)(b * 64 + cb) * 512 + tid;
  u64 gf = gload(fsrc);
  for (unsigned spin = 0; !__all(gok(gf, tag)); ) {
    if (++spin > SPIN_CAP) break;
    __builtin_amdgcn_s_sleep(2);
    gf = gload(fsrc);
  }
  {
    float r = dec24(gf);
    r += dpp<ROR8>(r);
    r = sum_rows(r);
    if (lane < 8) ((float*)(lds + L_R3))[w * 8 + lane] = r;
  }
  WG_BARRIER();
  if (tid < 8) {
    const float* r3 = (const float*)(lds + L_R3);
    const float* m2 = (const float*)(lds + L_M2);
    float st = 0.f, mt = 0.f;
#pragma unroll
    for (int ww = 0; ww < 8; ++ww) { st += r3[ww * 8 + tid]; mt += m2[ww * 8 + tid]; }
    const float mm = rbf(rbf(mt) + bflo(b2o));
    const float gt = rbf(mm / (1.0f + __expf(-mm)));
    const float o = bflo(xo) + rbf(rbf(st) * gt);
    p.out[b * DM + cb * 8 + tid] = (u16)f2bf(o);
    if (bid == 0 && tid == 0) __hip_atomic_store((GAS u64*)(ws + G_EPOCH), tag40 << 24, RLX_AGENT);
  }
}

}

extern "C" void kernel_launch(void* const* d_in, const int* in_sizes, int n_in,
                              void* d_out, int out_size, void* d_ws, size_t ws_size,
                              hipStream_t stream) {
  Params p;
  p.x = (const u16*)d_in[0]; p.k = (const u16*)d_in[1]; p.v = (const u16*)d_in[2];
  p.wq = (const u16*)d_in[3]; p.wo = (const u16*)d_in[4];
  p.w1 = (const u16*)d_in[6]; p.b1 = (const u16*)d_in[7]; p.w2 = (const u16*)d_in[8]; p.b2 = (const u16*)d_in[9];
  p.out = (u16*)d_out; p.ws = (u64*)d_ws;
  hipLaunchKernelGGL(fused_decode, dim3(NWG), dim3(NTHR), 0, stream, p);
}
```

```cpp
#include <hip/hip_runtime.h>
#include <hip/hip_bf16.h>
#include <stdint.h>

namespace {

typedef unsigned v4u __attribute__((ext_vector_type(4)));
typedef unsigned v2u __attribute__((ext_vector_type(2)));
typedef float f32x4 __attribute__((ext_vector_type(4)));
typedef short bf16x8 __attribute__((ext_vector_type(8)));
typedef unsigned long long u64;
typedef unsigned short u16;
#define GAS __attribute__((address_space(1)))
#define RLX_AGENT __ATOMIC_RELAXED, __HIP_MEMORY_SCOPE_AGENT

constexpr int DM = 512, SK = 16384, FF = 1792;
constexpr int NWG = 256, NTHR = 512;
constexpr int W = 32;
constexpr int RSRC_W3 = 0x00020000;
constexpr int AUX_NT = 2, AUX_SC1 = 16;
#define AUX_W 0
#define AUX_G 16
#define PSLEEP 2
constexpr unsigned SPIN_CAP = 1u << 16;

constexpr size_t G_EPOCH = 0;
constexpr size_t G_QX = 512;
constexpr size_t G_HX = G_QX + 4 * 4096;
constexpr size_t G_PX = G_HX + 4 * 2048;
constexpr size_t G_PS = G_PX + 32 * 8 * 512;
constexpr size_t G_SX = G_PS + 32 * 8 * 16;
constexpr size_t G_END = G_SX + 4 * 64 * 64 * 8;

constexpr int L_XS = 0, L_YS = 8192, L_RED = 16384, L_REDH = 18432, L_QS = 20480, L_OM = 24576, L_SM = 40960,
              L_SL = L_SM + 128, L_CV = 41472, L_CM = L_CV + 2048, L_CL = L_CM + 32, L_M2 = L_CM + 64,
              L_R3 = L_M2 + 256, L_FA = L_R3 + 256, L_FB = L_FA + 16, L_FC = L_FB + 16, L_FD = L_FC + 16,
              L_FQ = L_FD + 16, L_END = L_FA + 128;
constexpr int LDS_BYTES = 96 * 1024;
static_assert(L_END <= LDS_BYTES, "lds");

struct Params {
  const u16* x; const u16* k; const u16* v; const u16* wq; const u16* wo;
  const u16* w1; const u16* b1; const u16* w2; const u16* b2;
  u16* out; u64* ws;
};

__device__ __forceinline__ float asf(unsigned u) { return __builtin_bit_cast(float, u); }
__device__ __forceinline__ unsigned asu(float f) { return __builtin_bit_cast(unsigned, f); }
__device__ __forceinline__ float bflo(unsigned u) { return asf(u << 16); }
__device__ __forceinline__ float bfhi(unsigned u) { return asf(u & 0xffff0000u); }
__device__ __forceinline__ unsigned f2bf(float f) { return (unsigned)__builtin_bit_cast(u16, (__bf16)f); }
__device__ __forceinline__ float rbf(float f) { return (float)(__bf16)f; }
__device__ __forceinline__ unsigned cvtpk(float lo, float hi) {
  typedef __bf16 bf2 __attribute__((ext_vector_type(2)));
  bf2 r; r.x = (__bf16)lo; r.y = (__bf16)hi;
  return __builtin_bit_cast(unsigned, r);
}
template <int CTRL> __device__ __forceinline__ float dpp(float x) {
  return __builtin_bit_cast(float, __builtin_amdgcn_mov_dpp(__builtin_bit_cast(int, x), CTRL, 0xf, 0xf, true));
}
constexpr int XOR1 = 0xB1, XOR2 = 0x4E, HMIR = 0x141, MIR = 0x140, ROR8 = 0x128;
__device__ __forceinline__ float tr32(float a, float b) {
  auto r = __builtin_amdgcn_permlane32_swap(asu(a), asu(b), false, false);
  return asf(r[0]) + asf(r[1]);
}
__device__ __forceinline__ float tr16(float a, float b) {
  auto r = __builtin_amdgcn_permlane16_swap(asu(a), asu(b), false, false);
  return asf(r[0]) + asf(r[1]);
}
__device__ __forceinline__ float max_rows(float x) {
  auto s = __builtin_amdgcn_permlane16_swap(asu(x), asu(x), false, false);
  x = fmaxf(asf(s[0]), asf(s[1]));
  auto t = __builtin_amdgcn_permlane32_swap(asu(x), asu(x), false, false);
  return fmaxf(asf(t[0]), asf(t[1]));
}
__device__ __forceinline__ float sum_rows(float x) { x = tr16(x, x); return tr32(x, x); }
__device__ __forceinline__ float wave_sum(float x) {
  x += dpp<XOR1>(x); x += dpp<XOR2>(x); x += dpp<HMIR>(x); x += dpp<MIR>(x);
  return sum_rows(x);
}
__device__ __forceinline__ float treduce8(const float (&v)[8], int lane) {
  const float u0 = tr32(v[0], v[4]), u1 = tr32(v[1], v[5]), u2 = tr32(v[2], v[6]), u3 = tr32(v[3], v[7]);
  const float w0 = tr16(u0, u2), w1 = tr16(u1, u3);
  const float t0 = w0 + dpp<ROR8>(w0), t1 = w1 + dpp<ROR8>(w1);
  return (lane & 8) ? t1 : t0;
}
__device__ __forceinline__ void ln_row(const float (&v)[8], float (&o)[8]) {
  float s = 0.f;
#pragma unroll
  for (int i = 0; i < 8; ++i) s += v[i];
  s = wave_sum(s);
  const float mean = s * (1.0f / DM);
  float ss = 0.f;
#pragma unroll
  for (int i = 0; i < 8; ++i) { const float d = v[i] - mean; ss = fmaf(d, d, ss); }
  ss = wave_sum(ss);
  const float var_b = rbf(ss * (1.0f / DM)), m_b = rbf(mean);
  const float den = rbf(sqrtf(rbf(var_b + 1e-5f)));
#pragma unroll
  for (int i = 0; i < 8; ++i) o[i] = rbf(rbf(v[i] - m_b) / den);
}
#define WG_BARRIER() asm volatile("s_waitcnt lgkmcnt(0)\n\ts_barrier" ::: "memory")
#define SCHED() __builtin_amdgcn_sched_barrier(0)
#define STAMP(k) do {} while (0)

struct Tag { unsigned lo8, hi; };
__device__ __forceinline__ void gstore(u64* p, unsigned payload24, Tag t) {
  __hip_atomic_store((GAS u64*)p, ((u64)t.hi << 32) | (u64)(payload24 | t.lo8), RLX_AGENT);
}
__device__ __forceinline__ u64 gload(const u64* p) { return __hip_atomic_load((const GAS u64*)p, RLX_AGENT); }
__device__ __forceinline__ bool gok(u64 g, Tag t) {
  return (unsigned)(g >> 32) == t.hi && ((unsigned)g & 0xff000000u) == t.lo8;
}
__device__ __forceinline__ unsigned enc24(float f) { unsigned u = asu(f); u += 0x7fu + ((u >> 8) & 1u); return u >> 8; }
__device__ __forceinline__ float dec24(u64 g) { return asf((unsigned)g << 8); }

__device__ __forceinline__ f32x4 mfma(v4u a, v4u b, f32x4 c) {
  return __builtin_amdgcn_mfma_f32_16x16x32_bf16(__builtin_bit_cast(bf16x8, a), __builtin_bit_cast(bf16x8, b), c, 0, 0, 0);
}

__device__ __forceinline__ float gemv16(const unsigned (&wv)[16], const float* vec, int rg, int lane) {
  float acc[8];
#pragma unroll
  for (int bb = 0; bb < 4; ++bb) {
    float a0 = 0.f, a1 = 0.f;
#pragma unroll
    for (int q4 = 0; q4 < 4; ++q4) {
      const f32x4 x0 = *(const f32x4*)(vec + bb * DM + 16 * rg + 4 * q4);
#pragma unroll
      for (int r = 0; r < 4; ++r) { a0 = fmaf(x0[r], bflo(wv[4 * q4 + r]), a0); a1 = fmaf(x0[r], bfhi(wv[4 * q4 + r]), a1); }
    }
    acc[bb * 2] = a0; acc[bb * 2 + 1] = a1;
  }
  return treduce8(acc, lane);
}
__device__ __forceinline__ void lds_set(unsigned addr, unsigned v) {
  asm volatile("s_waitcnt lgkmcnt(0)\n\tds_write_b32 %0, %1" :: "v"(addr), "v"(v) : "memory");
}
__device__ __forceinline__ void lds_wait4(unsigned addr, unsigned v) {
#pragma nounroll
  for (unsigned spin = 0; spin < (1u << 22); ++spin) {
    v4u f;
    asm volatile("ds_read_b128 %0, %1\n\ts_waitcnt lgkmcnt(0)" : "=v"(f) : "v"(addr) : "memory");
    if (__builtin_amdgcn_readfirstlane((f.x == v) & (f.y == v) & (f.z == v) & (f.w == v))) break;
    __builtin_amdgcn_s_sleep(1);
  }
}
__device__ __forceinline__ void lds_wait1(unsigned addr, unsigned v) {
#pragma nounroll
  for (unsigned spin = 0; spin < (1u << 22); ++spin) {
    unsigned f;
    asm volatile("ds_read_b32 %0, %1\n\ts_waitcnt lgkmcnt(0)" : "=v"(f) : "v"(addr) : "memory");
    if (__builtin_amdgcn_readfirstlane(f) == v) break;
    __builtin_amdgcn_s_sleep(1);
  }
}

__global__ void __launch_bounds__(NTHR, 2) fused_decode(Params p) {
  __shared__ __attribute__((aligned(16))) unsigned char lds[LDS_BYTES];
  const int tid = threadIdx.x, lane = tid & 63, w = __builtin_amdgcn_readfirstlane(tid >> 6);
  const int bid = blockIdx.x, g = bid & 7, j = bid >> 3, b = j >> 3, s = j & 7, pair = b * 8 + g, cb = g * 8 + s;
  u64* ws = p.ws;
  const unsigned lb = (unsigned)(size_t)(__attribute__((address_space(3))) unsigned char*)lds;
  STAMP(0);

  const u64 e_raw = gload(ws + G_EPOCH);
  if (tid < 32) ((unsigned*)(lds + L_FA))[tid] = 0u;
  WG_BARRIER();
  const size_t kvoff = ((size_t)pair * SK + s * 2048 + w * 32) * 128;
  const __amdgpu_buffer_rsrc_t rk = __builtin_amdgcn_make_buffer_rsrc((void*)(p.k + kvoff), 0, 7 * 65536 + 8192, RSRC_W3);
  const __amdgpu_buffer_rsrc_t rv = __builtin_amdgcn_make_buffer_rsrc((void*)(p.v + kvoff), 0, 7 * 65536 + 8192, RSRC_W3);
  const int koff = ((lane & 15) >> 2) * 256 + (lane & 3) * 16 + (lane >> 4) * 64, voff = lane * 16;
  v4u a[W];
#define ISSUE(i_) do { constexpr int I_ = (i_); if constexpr (I_ < 64) \
      a[I_ % W] = __builtin_amdgcn_raw_buffer_load_b128(rk, koff + (I_ & 3) * 1024, (I_ >> 3) * 65536 + ((I_ >> 2) & 1) * 4096, AUX_NT); \
    else if constexpr (I_ < 128) \
      a[I_ % W] = __builtin_amdgcn_raw_buffer_load_b128(rv, voff + (I_ & 3) * 1024, ((I_ - 64) >> 3) * 65536 + ((I_ >> 2) & 1) * 4096, AUX_NT); } while (0)
#define ISSUE4(i_) do { ISSUE((i_)); ISSUE((i_) + 1); ISSUE((i_) + 2); ISSUE((i_) + 3); } while (0)
#define ISSUE8(i_) do { ISSUE4((i_)); ISSUE4((i_) + 4); } while (0)
#define MAKE_TAG() \
  const u64 e_ = ((u64)__builtin_amdgcn_readfirstlane((unsigned)(e_raw >> 32)) << 32) | (u64)__builtin_amdgcn_readfirstlane((unsigned)e_raw); \
  const u64 tag40 = ((e_ >> 24) * 0x5DEECE66DULL + 0xBULL) & ((1ULL << 40) - 1); \
  Tag tag; tag.lo8 = (unsigned)(tag40 & 0xff) << 24; tag.hi = (unsigned)(tag40 >> 8); \
  const unsigned ltag = 1u;
  if (w < 4) {
    const v4u xr = AUX_W ? __builtin_nontemporal_load((const GAS v4u*)(p.x + w * DM + 8 * lane)) : *(const GAS v4u*)(p.x + w * DM + 8 * lane);
    const int cp = tid & 7, rg = tid >> 3;
    const bool hwg = j >= 18;
    const int f0 = 224 * g + 16 * (hwg ? j - 18 : 0);
    unsigned wqv[16], w1v[16];
    {
      const __amdgpu_buffer_rsrc_t rq = __builtin_amdgcn_make_buffer_rsrc((void*)(p.wq + g * 512 + 16 * j), 0, 512 * 8192 - 8192 + 32, RSRC_W3);
#pragma unroll
      for (int r = 0; r < 16; ++r) wqv[r] = __builtin_amdgcn_raw_buffer_load_b32(rq, rg * (16 * 8192) + cp * 4, r * 8192, AUX_W);
      const __amdgpu_buffer_rsrc_t r1 = __builtin_amdgcn_make_buffer_rsrc((void*)(p.w1 + f0), 0, hwg ? 511 * FF * 2 + 32 : 0, RSRC_W3);
#pragma unroll
      for (int r = 0; r < 16; ++r) w1v[r] = __builtin_amdgcn_raw_buffer_load_b32(r1, rg * (16 * FF * 2) + cp * 4, r * FF * 2, AUX_W);
    }
    const int ob = lane >> 4, ocol = 2 * (lane & 7) + ((lane >> 3) & 1);
    const unsigned b1v = p.b1[f0 + ocol];
    SCHED();
    float* xs = (float*)(lds + L_XS);
    float* ys = (float*)(lds + L_YS);
    float* red = (float*)(lds + L_RED);
    float* redh = (float*)(lds + L_REDH);
    float xv[8], xn[8];
#pragma unroll
    for (int i = 0; i < 4; ++i) { xv[2 * i] = bflo(xr[i]); xv[2 * i + 1] = bfhi(xr[i]); }
    ln_row(xv, xn);
    *(f32x4*)(xs + w * DM + 8 * lane) = f32x4{xn[0], xn[1], xn[2], xn[3]};
    *(f32x4*)(xs + w * DM + 8 * lane + 4) = f32x4{xn[4], xn[5], xn[6], xn[7]};
    MAKE_TAG();
    lds_set(lb + L_FA + 4 * w, ltag);
    STAMP(1);
    lds_wait4(lb + L_FA, ltag);
    red[w * 64 + lane] = gemv16(wqv, xs, rg, lane);
    lds_set(lb + L_FB + 4 * w, ltag);
    const __amdgpu_buffer_rsrc_t rqx = __builtin_amdgcn_make_buffer_rsrc((void*)(ws + G_QX + b * 4096 + g * 512), 0, 4096, RSRC_W3);
    v4u gq[4];
    if (w == 0) {
      lds_wait4(lb + L_FB, ltag);
      const float q = (red[lane] + red[64 + lane]) + (red[128 + lane] + red[192 + lane]);
      gstore(ws + G_QX + ob * 4096 + g * 512 + 16 * j + ocol, f2bf(q), tag);
      STAMP(2);
    }
    if (hwg) {
      float y[8];
      ln_row(xn, y);
      *(f32x4*)(ys + w * DM + 8 * lane) = f32x4{y[0], y[1], y[2], y[3]};
      *(f32x4*)(ys + w * DM + 8 * lane + 4) = f32x4{y[4], y[5], y[6], y[7]};
      lds_set(lb + L_FC + 4 * w, ltag);
      lds_wait4(lb + L_FC, ltag);
      redh[w * 64 + lane] = gemv16(w1v, ys, rg, lane);
      lds_set(lb + L_FD + 4 * w, ltag);
      if (w == 1) {
        lds_wait4(lb + L_FD, ltag);
        const float hp = (redh[lane] + redh[64 + lane]) + (redh[128 + lane] + redh[192 + lane]);
        const float pre = rbf(rbf(hp) + bflo(b1v));
        const float hv = 0.5f * pre * (1.0f + tanhf(0.7978845608028654f * (pre + 0.044715f * pre * pre * pre)));
        gstore(ws + G_HX + ob * 2048 + f0 + ocol, f2bf(hv), tag);
      }
    }
    if (w == 0) {
#pragma nounroll
      for (unsigned spin = 0; spin < SPIN_CAP; ++spin) {
        asm volatile("" ::: "memory");
        bool ok = true;
#pragma unroll
        for (int k = 0; k < 4; ++k) gq[k] = __builtin_amdgcn_raw_buffer_load_b128(rqx, lane * 16 + k * 1024, 0, AUX_G);
#pragma unroll
        for (int k = 0; k < 4; ++k)
          ok = ok & (gq[k].y == tag.hi) & (gq[k].w == tag.hi) & ((gq[k].x & 0xff000000u) == tag.lo8) & ((gq[k].z & 0xff000000u) == tag.lo8);
        if (__all(ok)) break;
        __builtin_amdgcn_s_sleep(1);
      }
#pragma unroll
      for (int k = 0; k < 4; ++k) ((unsigned*)(lds + L_QS))[64 * k + lane] = (gq[k].x & 0xffffu) | (gq[k].z << 16);
      lds_set(lb + L_FQ, ltag);
      STAMP(3);
    }
  }
  SCHED();
  ISSUE8(0); ISSUE8(8); ISSUE8(16); ISSUE8(24);
  static_assert(W == 32, "window issue code");
  SCHED();
  MAKE_TAG();
  lds_wait1(lb + L_FQ, ltag);
  STAMP(4);
  const v4u qf = *(const v4u*)(lds + L_QS + ((lane & 15) >> 2) * 256 + ((lane & 3) + 4 * (lane >> 4)) * 16);

  constexpr float C2 = 0.08838834764831845f * 1.4426950408889634f;
  float sc[64];
#define KSTEP(i_) do { const f32x4 d_ = mfma(a[(i_) % W], qf, f32x4{0.f, 0.f, 0.f, 0.f}); ISSUE((i_) + W); \
    sc[(i_)] = ((dpp<0x00>(d_[0]) + dpp<0x55>(d_[1])) + (dpp<0xAA>(d_[2]) + dpp<0xFF>(d_[3]))) * C2; } while (0)
#define KSTEP4(i_) do { KSTEP((i_)); KSTEP((i_) + 1); KSTEP((i_) + 2); KSTEP((i_) + 3); SCHED(); } while (0)
#define KSTEP16(i_) do { KSTEP4((i_)); KSTEP4((i_) + 4); KSTEP4((i_) + 8); KSTEP4((i_) + 12); } while (0)
  KSTEP16(0); KSTEP16(16); KSTEP16(32); KSTEP16(48);
  STAMP(5);

  float m = sc[0];
#pragma unroll
  for (int i = 1; i < 64; ++i) m = fmaxf(m, sc[i]);
  m = max_rows(m);
  float lsum = 0.f;
  v4u pk[8];
#pragma unroll
  for (int t = 0; t < 8; ++t) {
#pragma unroll
    for (int k = 0; k < 4; ++k) {
      const float p0 = __builtin_amdgcn_exp2f(sc[8 * t + 2 * k] - m), p1 = __builtin_amdgcn_exp2f(sc[8 * t + 2 * k + 1] - m);
      lsum += p0 + p1;
      pk[t][k] = cvtpk(p0, p1);
    }
  }
  lsum = sum_rows(lsum);
  SCHED();

  f32x4 acc[8];
#pragma unroll
  for (int i = 0; i < 8; ++i) acc[i] = f32x4{0.f, 0.f, 0.f, 0.f};
  v4u wov[8], w2v[4];
  u64 hg[4];
#define ABL_WO 0
#define ABL_W2 0
  const __amdgpu_buffer_rsrc_t rwo = __builtin_amdgcn_make_buffer_rsrc((void*)(p.wo + (size_t)(g * 512 + 64 * s + 8 * w) * DM), 0, ABL_WO ? 0 : 8192, RSRC_W3);
  const __amdgpu_buffer_rsrc_t rw2 = __builtin_amdgcn_make_buffer_rsrc((void*)p.w2, 0, ABL_W2 ? 0 : FF * DM * 2, RSRC_W3);
  const u64* hsrc = ws + G_HX + b * 2048 + tid;
#define VTILE(t_) do { constexpr int B_ = (64 + 8 * (t_)) % W; \
    _Pragma("unroll") for (int jj = 0; jj < 4; ++jj) { \
      v4u lo_, hi_; \
      _Pragma("unroll") for (int k2 = 0; k2 < 4; ++k2) { \
        lo_[k2] = __builtin_amdgcn_perm(a[B_ + 2 * k2 + 1][jj], a[B_ + 2 * k2][jj], 0x05040100u); \
        hi_[k2] = __builtin_amdgcn_perm(a[B_ + 2 * k2 + 1][jj], a[B_ + 2 * k2][jj], 0x07060302u); } \
      acc[2 * jj] = mfma(pk[(t_)], lo_, acc[2 * jj]); acc[2 * jj + 1] = mfma(pk[(t_)], hi_, acc[2 * jj + 1]); } \
    SCHED(); ISSUE8(64 + 8 * (t_) + W); SCHED(); } while (0)
  VTILE(0); VTILE(1); VTILE(2); VTILE(3);
  VTILE(4);
#pragma unroll
  for (int r = 0; r < 8; ++r) wov[r] = __builtin_amdgcn_raw_buffer_load_b128(rwo, voff + (r & 3) * 1024, (r >> 2) * 4096, AUX_W);
  SCHED();
  VTILE(5);
#pragma unroll
  for (int k = 0; k < 4; ++k) w2v[k] = __builtin_amdgcn_raw_buffer_load_b128(rw2, tid * 1024 + cb * 16, k * 512 * 1024, AUX_W);
#pragma unroll
  for (int k = 0; k < 4; ++k) hg[k] = gload(hsrc + 512 * k);
  SCHED();
  VTILE(6); VTILE(7);
  STAMP(6);

  float* om = (float*)(lds + L_OM);
  float* sm = (float*)(lds + L_SM);
  float* sl = (float*)(lds + L_SL);
  *(f32x4*)(om + w * 512 + lane * 8) = f32x4{acc[0][0], acc[1][0], acc[2][0], acc[3][0]};
  *(f32x4*)(om + w * 512 + lane * 8 + 4) = f32x4{acc[4][0], acc[5][0], acc[6][0], acc[7][0]};
  if (lane < 16 && (lane & 3) == 0) { sm[w * 4 + (lane >> 2)] = m; sl[w * 4 + (lane >> 2)] = lsum; }
  WG_BARRIER();
  {
    const int h = tid >> 7;
    float M = sm[h];
#pragma unroll
    for (int ww = 1; ww < 8; ++ww) M = fmaxf(M, sm[ww * 4 + h]);
    float O = 0.f, L = 0.f;
#pragma unroll
    for (int ww = 0; ww < 8; ++ww) {
      const float f = __builtin_amdgcn_exp2f(sm[ww * 4 + h] - M);
      O = fmaf(f, om[ww * 512 + tid], O); L = fmaf(f, sl[ww * 4 + h], L);
    }
    gstore(ws + G_PX + (size_t)(pair * 8 + s) * 512 + tid, enc24(O), tag);
    const int d = tid & 127;
    if (d < 4) {
      const unsigned u = (d & 2) ? asu(L) : asu(M);
      gstore(ws + G_PS + (size_t)(pair * 8 + s) * 16 + h * 4 + d, (d & 1) ? (u & 0xffffu) : (u >> 16), tag);
    }
  }
  STAMP(7);
  const u64* psrc = ws + G_PX + (size_t)(pair * 8 + w) * 512 + 64 * s + lane;
  const u64* ssrc = ws + G_PS + (size_t)(pair * 8 + w) * 16 + (s >> 1) * 4 + (lane & 3);
  u64 gp = gload(psrc), gs = gload(ssrc);
  const unsigned xo = p.x[b * DM + cb * 8 + (tid & 7)], b2o = p.b2[cb * 8 + (tid & 7)];
  SCHED();
  {
    const bool k3 = w < 4;
#define HOK() __all(gok(hg[0], tag) & gok(hg[1], tag) & gok(hg[2], tag) & (!k3 | gok(hg[3], tag)))
    if (!HOK()) {
      unsigned spin = 0;
      do { __builtin_amdgcn_s_sleep(PSLEEP);
#pragma unroll
        for (int k = 0; k < 4; ++k) hg[k] = gload(hsrc + 512 * k);
      } while (!HOK() && ++spin < SPIN_CAP);
    }
    float ma[8];
#pragma unroll
    for (int e = 0; e < 8; ++e) ma[e] = 0.f;
#pragma unroll
    for (int k = 0; k < 4; ++k) {
      const float hv = (k < 3 || k3) ? bflo((unsigned)hg[k]) : 0.f;
#pragma unroll
      for (int e = 0; e < 4; ++e) { ma[2 * e] = fmaf(hv, bflo(w2v[k][e]), ma[2 * e]); ma[2 * e + 1] = fmaf(hv, bfhi(w2v[k][e]), ma[2 * e + 1]); }
    }
    float r = treduce8(ma, lane);
    r += dpp<XOR1>(r); r += dpp<XOR2>(r); r += dpp<HMIR>(r);
    if ((lane & 7) == 0) ((float*)(lds + L_M2))[w * 8 + (lane >> 3)] = r;
  }
  if (!__all(gok(gp, tag) && gok(gs, tag))) {
    unsigned spin = 0;
    do { __builtin_amdgcn_s_sleep(PSLEEP); gp = gload(psrc); gs = gload(ssrc); } while (!__all(gok(gp, tag) && gok(gs, tag)) && ++spin < SPIN_CAP);
  }
  STAMP(8);
  float* cv = (float*)(lds + L_CV);
  cv[w * 64 + lane] = dec24(gp);
  if (lane < 4) ((u16*)(lds + ((lane & 2) ? L_CL : L_CM)))[w * 2 + ((lane & 1) ^ 1)] = (u16)gs;
  WG_BARRIER();
  {
    const float* cm = (const float*)(lds + L_CM);
    const float* cl = (const float*)(lds + L_CL);
    float M = cm[0];
#pragma unroll
    for (int ww = 1; ww < 8; ++ww) M = fmaxf(M, cm[ww]);
    float O = 0.f, L = 0.f;
#pragma unroll
    for (int ww = 0; ww < 8; ++ww) {
      const float f = __builtin_amdgcn_exp2f(cm[ww] - M);
      O = fmaf(f, cv[ww * 64 + lane], O); L = fmaf(f, cl[ww], L);
    }
    const float o = rbf(O / L);
    float sa[8];
#pragma unroll
    for (int e = 0; e < 8; ++e) sa[e] = 0.f;
#pragma unroll
    for (int r = 0; r < 8; ++r) {
      const float ov = asf(__builtin_amdgcn_readlane(asu(o), 8 * w + r));
#pragma unroll
      for (int e = 0; e < 4; ++e) { sa[2 * e] = fmaf(ov, bflo(wov[r][e]), sa[2 * e]); sa[2 * e + 1] = fmaf(ov, bfhi(wov[r][e]), sa[2 * e + 1]); }
    }
    *(f32x4*)(om + w * 512 + lane * 8) = f32x4{sa[0], sa[1], sa[2], sa[3]};
    *(f32x4*)(om + w * 512 + lane * 8 + 4) = f32x4{sa[4], sa[5], sa[6], sa[7]};
  }
  WG_BARRIER();
  {
    float sp = 0.f;
#pragma unroll
    for (int ww = 0; ww < 8; ++ww) sp += om[ww * 512 + tid];
    gstore(ws + G_SX + ((size_t)(b * 64 + (tid >> 3)) * 64 + cb) * 8 + (tid & 7), enc24(sp), tag);
  }
  STAMP(9);
  const u64* fsrc = ws + G_SX + (size_t)(b * 64 + cb) * 512 + tid;
  u64 gf = gload(fsrc);
  for (unsigned spin = 0; !__all(gok(gf, tag)); ) {
    if (++spin > SPIN_CAP) break;
    __builtin_amdgcn_s_sleep(PSLEEP);
    gf = gload(fsrc);
  }
  STAMP(10);
  {
    float r = dec24(gf);
    r += dpp<ROR8>(r);
    r = sum_rows(r);
    if (lane < 8) ((float*)(lds + L_R3))[w * 8 + lane] = r;
  }
  WG_BARRIER();
  if (tid < 8) {
    const float* r3 = (const float*)(lds + L_R3);
    const float* m2 = (const float*)(lds + L_M2);
    float st = 0.f, mt = 0.f;
#pragma unroll
    for (int ww = 0; ww < 8; ++ww) { st += r3[ww * 8 + tid]; mt += m2[ww * 8 + tid]; }
    const float mm = rbf(rbf(mt) + bflo(b2o));
    const float gt = rbf(mm / (1.0f + __expf(-mm)));
    const float o = bflo(xo) + rbf(rbf(st) * gt);
    p.out[b * DM + cb * 8 + tid] = (u16)f2bf(o);
    if (bid == 0 && tid == 0) __hip_atomic_store((GAS u64*)(ws + G_EPOCH), tag40 << 24, RLX_AGENT);
  }
}

}

extern "C" void kernel_launch(void* const* d_in, const int* in_sizes, int n_in,
                              void* d_out, int out_size, void* d_ws, size_t ws_size,
                              hipStream_t stream) {
  Params p;
  p.x = (const u16*)d_in[0]; p.k = (const u16*)d_in[1]; p.v = (const u16*)d_in[2];
  p.wq = (const u16*)d_in[3]; p.wo = (const u16*)d_in[4];
  p.w1 = (const u16*)d_in[6]; p.b1 = (const u16*)d_in[7]; p.w2 = (const u16*)d_in[8]; p.b2 = (const u16*)d_in[9];
  p.out = (u16*)d_out; p.ws = (u64*)d_ws;
  hipLaunchKernelGGL(fused_decode, dim3(NWG), dim3(NTHR), 0, stream, p);
}
```

```cpp
#include <hip/hip_runtime.h>
#include <hip/hip_bf16.h>
#include <stdint.h>

namespace {

typedef unsigned v4u __attribute__((ext_vector_type(4)));
typedef unsigned v2u __attribute__((ext_vector_type(2)));
typedef float f32x4 __attribute__((ext_vector_type(4)));
typedef short bf16x8 __attribute__((ext_vector_type(8)));
typedef unsigned long long u64;
typedef unsigned short u16;
#define GAS __attribute__((address_space(1)))
#define RLX_AGENT __ATOMIC_RELAXED, __HIP_MEMORY_SCOPE_AGENT

constexpr int DM = 512, SK = 16384, FF = 1792;
constexpr int NWG = 256, NTHR = 512;
constexpr int W = 32;
constexpr int RSRC_W3 = 0x00020000;
constexpr int AUX_NT = 2, AUX_SC1 = 16;
#define AUX_W 0
#define AUX_K 2
#define AUX_V 0
#define AUX_G 16
#define PSLEEP 2
constexpr unsigned SPIN_CAP = 1u << 16;

constexpr size_t G_EPOCH = 0;
constexpr size_t G_QX = 512;
constexpr size_t G_HX = G_QX + 4 * 4096;
constexpr size_t G_PX = G_HX + 4 * 2048;
constexpr size_t G_PS = G_PX + 32 * 8 * 512;
constexpr size_t G_SX = G_PS + 32 * 8 * 16;
constexpr size_t G_END = G_SX + 4 * 64 * 64 * 8;

constexpr int L_XS = 0, L_YS = 8192, L_RED = 16384, L_REDH = 18432, L_QS = 20480, L_OM = 24576, L_SM = 40960,
              L_SL = L_SM + 128, L_CV = 41472, L_CM = L_CV + 2048, L_CL = L_CM + 32, L_M2 = L_CM + 64,
              L_R3 = L_M2 + 256, L_FA = L_R3 + 256, L_FB = L_FA + 16, L_FC = L_FB + 16, L_FD = L_FC + 16,
              L_FQ = L_FD + 16, L_END = L_FA + 128;
constexpr int LDS_BYTES = 96 * 1024;
static_assert(L_END <= LDS_BYTES, "lds");

struct Params {
  const u16* x; const u16* k; const u16* v; const u16* wq; const u16* wo;
  const u16* w1; const u16* b1; const u16* w2; const u16* b2;
  u16* out; u64* ws;
};

__device__ __forceinline__ float asf(unsigned u) { return __builtin_bit_cast(float, u); }
__device__ __forceinline__ unsigned asu(float f) { return __builtin_bit_cast(unsigned, f); }
__device__ __forceinline__ float bflo(unsigned u) { return asf(u << 16); }
__device__ __forceinline__ float bfhi(unsigned u) { return asf(u & 0xffff0000u); }
__device__ __forceinline__ unsigned f2bf(float f) { return (unsigned)__builtin_bit_cast(u16, (__bf16)f); }
__device__ __forceinline__ float rbf(float f) { return (float)(__bf16)f; }
__device__ __forceinline__ unsigned cvtpk(float lo, float hi) {
  typedef __bf16 bf2 __attribute__((ext_vector_type(2)));
  bf2 r; r.x = (__bf16)lo; r.y = (__bf16)hi;
  return __builtin_bit_cast(unsigned, r);
}
template <int CTRL> __device__ __forceinline__ float dpp(float x) {
  return __builtin_bit_cast(float, __builtin_amdgcn_mov_dpp(__builtin_bit_cast(int, x), CTRL, 0xf, 0xf, true));
}
constexpr int XOR1 = 0xB1, XOR2 = 0x4E, HMIR = 0x141, MIR = 0x140, ROR8 = 0x128;
__device__ __forceinline__ float tr32(float a, float b) {
  auto r = __builtin_amdgcn_permlane32_swap(asu(a), asu(b), false, false);
  return asf(r[0]) + asf(r[1]);
}
__device__ __forceinline__ float tr16(float a, float b) {
  auto r = __builtin_amdgcn_permlane16_swap(asu(a), asu(b), false, false);
  return asf(r[0]) + asf(r[1]);
}
__device__ __forceinline__ float max_rows(float x) {
  auto s = __builtin_amdgcn_permlane16_swap(asu(x), asu(x), false, false);
  x = fmaxf(asf(s[0]), asf(s[1]));
  auto t = __builtin_amdgcn_permlane32_swap(asu(x), asu(x), false, false);
  return fmaxf(asf(t[0]), asf(t[1]));
}
__device__ __forceinline__ float sum_rows(float x) { x = tr16(x, x); return tr32(x, x); }
__device__ __forceinline__ float wave_sum(float x) {
  x += dpp<XOR1>(x); x += dpp<XOR2>(x); x += dpp<HMIR>(x); x += dpp<MIR>(x);
  return sum_rows(x);
}
__device__ __forceinline__ float treduce8(const float (&v)[8], int lane) {
  const float u0 = tr32(v[0], v[4]), u1 = tr32(v[1], v[5]), u2 = tr32(v[2], v[6]), u3 = tr32(v[3], v[7]);
  const float w0 = tr16(u0, u2), w1 = tr16(u1, u3);
  const float t0 = w0 + dpp<ROR8>(w0), t1 = w1 + dpp<ROR8>(w1);
  return (lane & 8) ? t1 : t0;
}
__device__ __forceinline__ void ln_row(const float (&v)[8], float (&o)[8]) {
  float s = 0.f;
#pragma unroll
  for (int i = 0; i < 8; ++i) s += v[i];
  s = wave_sum(s);
  const float mean = s * (1.0f / DM);
  float ss = 0.f;
#pragma unroll
  for (int i = 0; i < 8; ++i) { const float d = v[i] - mean; ss = fmaf(d, d, ss); }
  ss = wave_sum(ss);
  const float var_b = rbf(ss * (1.0f / DM)), m_b = rbf(mean);
  const float den = rbf(sqrtf(rbf(var_b + 1e-5f)));
#pragma unroll
  for (int i = 0; i < 8; ++i) o[i] = rbf(rbf(v[i] - m_b) / den);
}
#define WG_BARRIER() asm volatile("s_waitcnt lgkmcnt(0)\n\ts_barrier" ::: "memory")
#define SCHED() __builtin_amdgcn_sched_barrier(0)
#define STAMP(k) do {} while (0)

struct Tag { unsigned lo8, hi; };
__device__ __forceinline__ void gstore(u64* p, unsigned payload24, Tag t) {
  __hip_atomic_store((GAS u64*)p, ((u64)t.hi << 32) | (u64)(payload24 | t.lo8), RLX_AGENT);
}
__device__ __forceinline__ u64 gload(const u64* p) { return __hip_atomic_load((const GAS u64*)p, RLX_AGENT); }
__device__ __forceinline__ bool gok(u64 g, Tag t) {
  return (unsigned)(g >> 32) == t.hi && ((unsigned)g & 0xff000000u) == t.lo8;
}
__device__ __forceinline__ unsigned enc24(float f) { unsigned u = asu(f); u += 0x7fu + ((u >> 8) & 1u); return u >> 8; }
__device__ __forceinline__ float dec24(u64 g) { return asf((unsigned)g << 8); }

__device__ __forceinline__ f32x4 mfma(v4u a, v4u b, f32x4 c) {
  return __builtin_amdgcn_mfma_f32_16x16x32_bf16(__builtin_bit_cast(bf16x8, a), __builtin_bit_cast(bf16x8, b), c, 0, 0, 0);
}

__device__ __forceinline__ float gemv16(const unsigned (&wv)[16], const float* vec, int rg, int lane) {
  float acc[8];
#pragma unroll
  for (int bb = 0; bb < 4; ++bb) {
    float a0 = 0.f, a1 = 0.f;
#pragma unroll
    for (int q4 = 0; q4 < 4; ++q4) {
      const f32x4 x0 = *(const f32x4*)(vec + bb * DM + 16 * rg + 4 * q4);
#pragma unroll
      for (int r = 0; r < 4; ++r) { a0 = fmaf(x0[r], bflo(wv[4 * q4 + r]), a0); a1 = fmaf(x0[r], bfhi(wv[4 * q4 + r]), a1); }
    }
    acc[bb * 2] = a0; acc[bb * 2 + 1] = a1;
  }
  return treduce8(acc, lane);
}
__device__ __forceinline__ void lds_set(unsigned addr, unsigned v) {
  asm volatile("s_waitcnt lgkmcnt(0)\n\tds_write_b32 %0, %1" :: "v"(addr), "v"(v) : "memory");
}
__device__ __forceinline__ void lds_wait4(unsigned addr, unsigned v) {
#pragma nounroll
  for (unsigned spin = 0; spin < (1u << 22); ++spin) {
    v4u f;
    asm volatile("ds_read_b128 %0, %1\n\ts_waitcnt lgkmcnt(0)" : "=v"(f) : "v"(addr) : "memory");
    if (__builtin_amdgcn_readfirstlane((f.x == v) & (f.y == v) & (f.z == v) & (f.w == v))) break;
    __builtin_amdgcn_s_sleep(1);
  }
}
__device__ __forceinline__ void lds_wait1(unsigned addr, unsigned v) {
#pragma nounroll
  for (unsigned spin = 0; spin < (1u << 22); ++spin) {
    unsigned f;
    asm volatile("ds_read_b32 %0, %1\n\ts_waitcnt lgkmcnt(0)" : "=v"(f) : "v"(addr) : "memory");
    if (__builtin_amdgcn_readfirstlane(f) == v) break;
    __builtin_amdgcn_s_sleep(1);
  }
}

__global__ void __launch_bounds__(NTHR, 2) fused_decode(Params p) {
  __shared__ __attribute__((aligned(16))) unsigned char lds[LDS_BYTES];
  const int tid = threadIdx.x, lane = tid & 63, w = __builtin_amdgcn_readfirstlane(tid >> 6);
  const int bid = blockIdx.x, g = bid & 7, j = bid >> 3, b = j >> 3, s = j & 7, pair = b * 8 + g, cb = g * 8 + s;
  u64* ws = p.ws;
  const unsigned lb = (unsigned)(size_t)(__attribute__((address_space(3))) unsigned char*)lds;
  STAMP(0);

  const u64 e_raw = gload(ws + G_EPOCH);
  if (tid < 32) ((unsigned*)(lds + L_FA))[tid] = 0u;
  WG_BARRIER();
  const size_t kvoff = ((size_t)pair * SK + s * 2048 + w * 32) * 128;
  const __amdgpu_buffer_rsrc_t rk = __builtin_amdgcn_make_buffer_rsrc((void*)(p.k + kvoff), 0, 7 * 65536 + 8192, RSRC_W3);
  const __amdgpu_buffer_rsrc_t rv = __builtin_amdgcn_make_buffer_rsrc((void*)(p.v + kvoff), 0, 7 * 65536 + 8192, RSRC_W3);
  const int koff = ((lane & 15) >> 2) * 256 + (lane & 3) * 16 + (lane >> 4) * 64, voff = lane * 16;
  v4u a[W];
#define ISSUE(i_) do { constexpr int I_ = (i_); if constexpr (I_ < 64) \
      a[I_ % W] = __builtin_amdgcn_raw_buffer_load_b128(rk, koff + (I_ & 3) * 1024, (I_ >> 3) * 65536 + ((I_ >> 2) & 1) * 4096, AUX_K); \
    else if constexpr (I_ < 128) \
      a[I_ % W] = __builtin_amdgcn_raw_buffer_load_b128(rv, voff + (I_ & 3) * 1024, ((I_ - 64) >> 3) * 65536 + ((I_ >> 2) & 1) * 4096, AUX_V); } while (0)
#define ISSUE4(i_) do { ISSUE((i_)); ISSUE((i_) + 1); ISSUE((i_) + 2); ISSUE((i_) + 3); } while (0)
#define ISSUE8(i_) do { ISSUE4((i_)); ISSUE4((i_) + 4); } while (0)
#define MAKE_TAG() \
  const u64 e_ = ((u64)__builtin_amdgcn_readfirstlane((unsigned)(e_raw >> 32)) << 32) | (u64)__builtin_amdgcn_readfirstlane((unsigned)e_raw); \
  const u64 tag40 = ((e_ >> 24) * 0x5DEECE66DULL + 0xBULL) & ((1ULL << 40) - 1); \
  Tag tag; tag.lo8 = (unsigned)(tag40 & 0xff) << 24; tag.hi = (unsigned)(tag40 >> 8); \
  const unsigned ltag = 1u;
  if (w < 4) {
    const v4u xr = AUX_W ? __builtin_nontemporal_load((const GAS v4u*)(p.x + w * DM + 8 * lane)) : *(const GAS v4u*)(p.x + w * DM + 8 * lane);
    const int cp = tid & 7, rg = tid >> 3;
    const bool hwg = j >= 18;
    const int f0 = 224 * g + 16 * (hwg ? j - 18 : 0);
    unsigned wqv[16], w1v[16];
    {
      const __amdgpu_buffer_rsrc_t rq = __builtin_amdgcn_make_buffer_rsrc((void*)(p.wq + g * 512 + 16 * j), 0, 512 * 8192 - 8192 + 32, RSRC_W3);
#pragma unroll
      for (int r = 0; r < 16; ++r) wqv[r] = __builtin_amdgcn_raw_buffer_load_b32(rq, rg * (16 * 8192) + cp * 4, r * 8192, AUX_W);
      const __amdgpu_buffer_rsrc_t r1 = __builtin_amdgcn_make_buffer_rsrc((void*)(p.w1 + f0), 0, hwg ? 511 * FF * 2 + 32 : 0, RSRC_W3);
#pragma unroll
      for (int r = 0; r < 16; ++r) w1v[r] = __builtin_amdgcn_raw_buffer_load_b32(r1, rg * (16 * FF * 2) + cp * 4, r * FF * 2, AUX_W);
    }
    const int ob = lane >> 4, ocol = 2 * (lane & 7) + ((lane >> 3) & 1);
    const unsigned b1v = p.b1[f0 + ocol];
    SCHED();
    float* xs = (float*)(lds + L_XS);
    float* ys = (float*)(lds + L_YS);
    float* red = (float*)(lds + L_RED);
    float* redh = (float*)(lds + L_REDH);
    float xv[8], xn[8];
#pragma unroll
    for (int i = 0; i < 4; ++i) { xv[2 * i] = bflo(xr[i]); xv[2 * i + 1] = bfhi(xr[i]); }
    ln_row(xv, xn);
    *(f32x4*)(xs + w * DM + 8 * lane) = f32x4{xn[0], xn[1], xn[2], xn[3]};
    *(f32x4*)(xs + w * DM + 8 * lane + 4) = f32x4{xn[4], xn[5], xn[6], xn[7]};
    MAKE_TAG();
    lds_set(lb + L_FA + 4 * w, ltag);
    STAMP(1);
    lds_wait4(lb + L_FA, ltag);
    red[w * 64 + lane] = gemv16(wqv, xs, rg, lane);
    lds_set(lb + L_FB + 4 * w, ltag);
    const __amdgpu_buffer_rsrc_t rqx = __builtin_amdgcn_make_buffer_rsrc((void*)(ws + G_QX + b * 4096 + g * 512), 0, 4096, RSRC_W3);
    v4u gq[4];
    if (w == 0) {
      lds_wait4(lb + L_FB, ltag);
      const float q = (red[lane] + red[64 + lane]) + (red[128 + lane] + red[192 + lane]);
      gstore(ws + G_QX + ob * 4096 + g * 512 + 16 * j + ocol, f2bf(q), tag);
      STAMP(2);
    }
    if (hwg) {
      float y[8];
      ln_row(xn, y);
      *(f32x4*)(ys + w * DM + 8 * lane) = f32x4{y[0], y[1], y[2], y[3]};
      *(f32x4*)(ys + w * DM + 8 * lane + 4) = f32x4{y[4], y[5], y[6], y[7]};
      lds_set(lb + L_FC + 4 * w, ltag);
      lds_wait4(lb + L_FC, ltag);
      redh[w * 64 + lane] = gemv16(w1v, ys, rg, lane);
      lds_set(lb + L_FD + 4 * w, ltag);
      if (w == 1) {
        lds_wait4(lb + L_FD, ltag);
        const float hp = (redh[lane] + redh[64 + lane]) + (redh[128 + lane] + redh[192 + lane]);
        const float pre = rbf(rbf(hp) + bflo(b1v));
        const float hv = 0.5f * pre * (1.0f + tanhf(0.7978845608028654f * (pre + 0.044715f * pre * pre * pre)));
        gstore(ws + G_HX + ob * 2048 + f0 + ocol, f2bf(hv), tag);
      }
    }
    if (w == 0) {
#pragma nounroll
      for (unsigned spin = 0; spin < SPIN_CAP; ++spin) {
        asm volatile("" ::: "memory");
        bool ok = true;
#pragma unroll
        for (int k = 0; k < 4; ++k) gq[k] = __builtin_amdgcn_raw_buffer_load_b128(rqx, lane * 16 + k * 1024, 0, AUX_G);
#pragma unroll
        for (int k = 0; k < 4; ++k)
          ok = ok & (gq[k].y == tag.hi) & (gq[k].w == tag.hi) & ((gq[k].x & 0xff000000u) == tag.lo8) & ((gq[k].z & 0xff000000u) == tag.lo8);
        if (__all(ok)) break;
        __builtin_amdgcn_s_sleep(1);
      }
#pragma unroll
      for (int k = 0; k < 4; ++k) ((unsigned*)(lds + L_QS))[64 * k + lane] = (gq[k].x & 0xffffu) | (gq[k].z << 16);
      lds_set(lb + L_FQ, ltag);
      STAMP(3);
    }
  }
  SCHED();
  ISSUE8(0); ISSUE8(8); ISSUE8(16); ISSUE8(24);
  static_assert(W == 32, "window issue code");
  SCHED();
  MAKE_TAG();
  lds_wait1(lb + L_FQ, ltag);
  STAMP(4);
  const v4u qf = *(const v4u*)(lds + L_QS + ((lane & 15) >> 2) * 256 + ((lane & 3) + 4 * (lane >> 4)) * 16);

  constexpr float C2 = 0.08838834764831845f * 1.4426950408889634f;
  float sc[64];
#define KSTEP(i_) do { const f32x4 d_ = mfma(a[(i_) % W], qf, f32x4{0.f, 0.f, 0.f, 0.f}); ISSUE((i_) + W); \
    sc[(i_)] = ((dpp<0x00>(d_[0]) + dpp<0x55>(d_[1])) + (dpp<0xAA>(d_[2]) + dpp<0xFF>(d_[3]))) * C2; } while (0)
#define KSTEP4(i_) do { KSTEP((i_)); KSTEP((i_) + 1); KSTEP((i_) + 2); KSTEP((i_) + 3); SCHED(); } while (0)
#define KSTEP16(i_) do { KSTEP4((i_)); KSTEP4((i_) + 4); KSTEP4((i_) + 8); KSTEP4((i_) + 12); } while (0)
  KSTEP16(0); KSTEP16(16); KSTEP16(32); KSTEP16(48);
  STAMP(5);

  float m = sc[0];
#pragma unroll
  for (int i = 1; i < 64; ++i) m = fmaxf(m, sc[i]);
  m = max_rows(m);
  float lsum = 0.f;
  v4u pk[8];
#pragma unroll
  for (int t = 0; t < 8; ++t) {
#pragma unroll
    for (int k = 0; k < 4; ++k) {
      const float p0 = __builtin_amdgcn_exp2f(sc[8 * t + 2 * k] - m), p1 = __builtin_amdgcn_exp2f(sc[8 * t + 2 * k + 1] - m);
      lsum += p0 + p1;
      pk[t][k] = cvtpk(p0, p1);
    }
  }
  lsum = sum_rows(lsum);
  SCHED();

  f32x4 acc[8];
#pragma unroll
  for (int i = 0; i < 8; ++i) acc[i] = f32x4{0.f, 0.f, 0.f, 0.f};
  v4u wov[8], w2v[4];
  u64 hg[4];
#define ABL_WO 0
#define ABL_W2 0
  const __amdgpu_buffer_rsrc_t rwo = __builtin_amdgcn_make_buffer_rsrc((void*)(p.wo + (size_t)(g * 512 + 64 * s + 8 * w) * DM), 0, ABL_WO ? 0 : 8192, RSRC_W3);
  const __amdgpu_buffer_rsrc_t rw2 = __builtin_amdgcn_make_buffer_rsrc((void*)p.w2, 0, ABL_W2 ? 0 : FF * DM * 2, RSRC_W3);
  const u64* hsrc = ws + G_HX + b * 2048 + tid;
#define VTILE(t_) do { constexpr int B_ = (64 + 8 * (t_)) % W; \
    _Pragma("unroll") for (int jj = 0; jj < 4; ++jj) { \
      v4u lo_, hi_; \
      _Pragma("unroll") for (int k2 = 0; k2 < 4; ++k2) { \
        lo_[k2] = __builtin_amdgcn_perm(a[B_ + 2 * k2 + 1][jj], a[B_ + 2 * k2][jj], 0x05040100u); \
        hi_[k2] = __builtin_amdgcn_perm(a[B_ + 2 * k2 + 1][jj], a[B_ + 2 * k2][jj], 0x07060302u); } \
      acc[2 * jj] = mfma(pk[(t_)], lo_, acc[2 * jj]); acc[2 * jj + 1] = mfma(pk[(t_)], hi_, acc[2 * jj + 1]); } \
    SCHED(); ISSUE8(64 + 8 * (t_) + W); SCHED(); } while (0)
  VTILE(0); VTILE(1); VTILE(2); VTILE(3);
  VTILE(4);
#pragma unroll
  for (int r = 0; r < 8; ++r) wov[r] = __builtin_amdgcn_raw_buffer_load_b128(rwo, voff + (r & 3) * 1024, (r >> 2) * 4096, AUX_W);
  SCHED();
  VTILE(5);
#pragma unroll
  for (int k = 0; k < 4; ++k) w2v[k] = __builtin_amdgcn_raw_buffer_load_b128(rw2, tid * 1024 + cb * 16, k * 512 * 1024, AUX_W);
#pragma unroll
  for (int k = 0; k < 4; ++k) hg[k] = gload(hsrc + 512 * k);
  SCHED();
  VTILE(6); VTILE(7);
  STAMP(6);

  float* om = (float*)(lds + L_OM);
  float* sm = (float*)(lds + L_SM);
  float* sl = (float*)(lds + L_SL);
  *(f32x4*)(om + w * 512 + lane * 8) = f32x4{acc[0][0], acc[1][0], acc[2][0], acc[3][0]};
  *(f32x4*)(om + w * 512 + lane * 8 + 4) = f32x4{acc[4][0], acc[5][0], acc[6][0], acc[7][0]};
  if (lane < 16 && (lane & 3) == 0) { sm[w * 4 + (lane >> 2)] = m; sl[w * 4 + (lane >> 2)] = lsum; }
  WG_BARRIER();
  {
    const int h = tid >> 7;
    float M = sm[h];
#pragma unroll
    for (int ww = 1; ww < 8; ++ww) M = fmaxf(M, sm[ww * 4 + h]);
    float O = 0.f, L = 0.f;
#pragma unroll
    for (int ww = 0; ww < 8; ++ww) {
      const float f = __builtin_amdgcn_exp2f(sm[ww * 4 + h] - M);
      O = fmaf(f, om[ww * 512 + tid], O); L = fmaf(f, sl[ww * 4 + h], L);
    }
    gstore(ws + G_PX + (size_t)(pair * 8 + s) * 512 + tid, enc24(O), tag);
    const int d = tid & 127;
    if (d < 4) {
      const unsigned u = (d & 2) ? asu(L) : asu(M);
      gstore(ws + G_PS + (size_t)(pair * 8 + s) * 16 + h * 4 + d, (d & 1) ? (u & 0xffffu) : (u >> 16), tag);
    }
  }
  STAMP(7);
  const u64* psrc = ws + G_PX + (size_t)(pair * 8 + w) * 512 + 64 * s + lane;
  const u64* ssrc = ws + G_PS + (size_t)(pair * 8 + w) * 16 + (s >> 1) * 4 + (lane & 3);
  u64 gp = gload(psrc), gs = gload(ssrc);
  const unsigned xo = p.x[b * DM + cb * 8 + (tid & 7)], b2o = p.b2[cb * 8 + (tid & 7)];
  SCHED();
  {
    const bool k3 = w < 4;
#define HOK() __all(gok(hg[0], tag) & gok(hg[1], tag) & gok(hg[2], tag) & (!k3 | gok(hg[3], tag)))
    if (!HOK()) {
      unsigned spin = 0;
      do { __builtin_amdgcn_s_sleep(PSLEEP);
#pragma unroll
        for (int k = 0; k < 4; ++k) hg[k] = gload(hsrc + 512 * k);
      } while (!HOK() && ++spin < SPIN_CAP);
    }
    float ma[8];
#pragma unroll
    for (int e = 0; e < 8; ++e) ma[e] = 0.f;
#pragma unroll
    for (int k = 0; k < 4; ++k) {
      const float hv = (k < 3 || k3) ? bflo((unsigned)hg[k]) : 0.f;
#pragma unroll
      for (int e = 0; e < 4; ++e) { ma[2 * e] = fmaf(hv, bflo(w2v[k][e]), ma[2 * e]); ma[2 * e + 1] = fmaf(hv, bfhi(w2v[k][e]), ma[2 * e + 1]); }
    }
    float r = treduce8(ma, lane);
    r += dpp<XOR1>(r); r += dpp<XOR2>(r); r += dpp<HMIR>(r);
    if ((lane & 7) == 0) ((float*)(lds + L_M2))[w * 8 + (lane >> 3)] = r;
  }
  if (!__all(gok(gp, tag) && gok(gs, tag))) {
    unsigned spin = 0;
    do { __builtin_amdgcn_s_sleep(PSLEEP); gp = gload(psrc); gs = gload(ssrc); } while (!__all(gok(gp, tag) && gok(gs, tag)) && ++spin < SPIN_CAP);
  }
  STAMP(8);
  float* cv = (float*)(lds + L_CV);
  cv[w * 64 + lane] = dec24(gp);
  if (lane < 4) ((u16*)(lds + ((lane & 2) ? L_CL : L_CM)))[w * 2 + ((lane & 1) ^ 1)] = (u16)gs;
  WG_BARRIER();
  {
    const float* cm = (const float*)(lds + L_CM);
    const float* cl = (const float*)(lds + L_CL);
    float M = cm[0];
#pragma unroll
    for (int ww = 1; ww < 8; ++ww) M = fmaxf(M, cm[ww]);
    float O = 0.f, L = 0.f;
#pragma unroll
    for (int ww = 0; ww < 8; ++ww) {
      const float f = __builtin_amdgcn_exp2f(cm[ww] - M);
      O = fmaf(f, cv[ww * 64 + lane], O); L = fmaf(f, cl[ww], L);
    }
    const float o = rbf(O / L);
    float sa[8];
#pragma unroll
    for (int e = 0; e < 8; ++e) sa[e] = 0.f;
#pragma unroll
    for (int r = 0; r < 8; ++r) {
      const float ov = asf(__builtin_amdgcn_readlane(asu(o), 8 * w + r));
#pragma unroll
      for (int e = 0; e < 4; ++e) { sa[2 * e] = fmaf(ov, bflo(wov[r][e]), sa[2 * e]); sa[2 * e + 1] = fmaf(ov, bfhi(wov[r][e]), sa[2 * e + 1]); }
    }
    *(f32x4*)(om + w * 512 + lane * 8) = f32x4{sa[0], sa[1], sa[2], sa[3]};
    *(f32x4*)(om + w * 512 + lane * 8 + 4) = f32x4{sa[4], sa[5], sa[6], sa[7]};
  }
  WG_BARRIER();
  {
    float sp = 0.f;
#pragma unroll
    for (int ww = 0; ww < 8; ++ww) sp += om[ww * 512 + tid];
    gstore(ws + G_SX + ((size_t)(b * 64 + (tid >> 3)) * 64 + cb) * 8 + (tid & 7), enc24(sp), tag);
  }
  STAMP(9);
  const u64* fsrc = ws + G_SX + (size_t)(b * 64 + cb) * 512 + tid;
  u64 gf = gload(fsrc);
  for (unsigned spin = 0; !__all(gok(gf, tag)); ) {
    if (++spin > SPIN_CAP) break;
    __builtin_amdgcn_s_sleep(PSLEEP);
    gf = gload(fsrc);
  }
  STAMP(10);
  {
    float r = dec24(gf);
    r += dpp<ROR8>(r);
    r = sum_rows(r);
    if (lane < 8) ((float*)(lds + L_R3))[w * 8 + lane] = r;
  }
  WG_BARRIER();
  if (tid < 8) {
    const float* r3 = (const float*)(lds + L_R3);
    const float* m2 = (const float*)(lds + L_M2);
    float st = 0.f, mt = 0.f;
#pragma unroll
    for (int ww = 0; ww < 8; ++ww) { st += r3[ww * 8 + tid]; mt += m2[ww * 8 + tid]; }
    const float mm = rbf(rbf(mt) + bflo(b2o));
    const float gt = rbf(mm / (1.0f + __expf(-mm)));
    const float o = bflo(xo) + rbf(rbf(st) * gt);
    p.out[b * DM + cb * 8 + tid] = (u16)f2bf(o);
    if (bid == 0 && tid == 0) __hip_atomic_store((GAS u64*)(ws + G_EPOCH), tag40 << 24, RLX_AGENT);
  }
}

}

extern "C" void kernel_launch(void* const* d_in, const int* in_sizes, int n_in,
                              void* d_out, int out_size, void* d_ws, size_t ws_size,
                              hipStream_t stream) {
  Params p;
  p.x = (const u16*)d_in[0]; p.k = (const u16*)d_in[1]; p.v = (const u16*)d_in[2];
  p.wq = (const u16*)d_in[3]; p.wo = (const u16*)d_in[4];
  p.w1 = (const u16*)d_in[6]; p.b1 = (const u16*)d_in[7]; p.w2 = (const u16*)d_in[8]; p.b2 = (const u16*)d_in[9];
  p.out = (u16*)d_out; p.ws = (u64*)d_ws;
  hipLaunchKernelGGL(fused_decode, dim3(NWG), dim3(NTHR), 0, stream, p);
}
```

```cpp
#include <hip/hip_runtime.h>
#include <hip/hip_bf16.h>
#include <stdint.h>

namespace {

typedef unsigned v4u __attribute__((ext_vector_type(4)));
typedef unsigned v2u __attribute__((ext_vector_type(2)));
typedef float f32x4 __attribute__((ext_vector_type(4)));
typedef short bf16x8 __attribute__((ext_vector_type(8)));
typedef unsigned long long u64;
typedef unsigned short u16;
#define GAS __attribute__((address_space(1)))
#define RLX_AGENT __ATOMIC_RELAXED, __HIP_MEMORY_SCOPE_AGENT

constexpr int DM = 512, SK = 16384, FF = 1792;
constexpr int NWG = 256, NTHR = 512;
constexpr int W = 32;
constexpr int RSRC_W3 = 0x00020000;
constexpr int AUX_NT = 2, AUX_SC1 = 16;
#define AUX_W 0
#define AUX_K 2
#define AUX_V 2
#define TOPWIN 0
#define PW 1
#define ABL_RED 0
#define DRY 0
#define AUX_G 16
#define PSLEEP 2
constexpr unsigned SPIN_CAP = 1u << 16;

constexpr size_t G_EPOCH = 0;
constexpr size_t G_QX = 512;
constexpr size_t G_PS = G_QX + 4 * 4096;
constexpr size_t G_PX = G_PS + 32 * 8 * 16;
constexpr size_t G_SX = G_PX + 32 * 8 * 512;
constexpr size_t G_HX = G_SX + 4 * 64 * 64 * 8;
constexpr size_t G_MX = G_HX + 4 * 2048;
constexpr size_t G_END = G_MX + 4 * 64 * 32 * 8;
constexpr size_t G_SHADOW = 524288;
static_assert(G_END <= G_SHADOW, "ws");

constexpr int WO_LD = 1040;
constexpr int L_OM = 0;
constexpr int L_XS = 16384, L_YS = L_XS + 8192;
constexpr int L_RED = 32768, L_REDH = L_RED + 1024, L_QS = L_REDH + 1024, L_SM = L_QS + 1024, L_SL = L_SM + 128,
              L_GATE = L_SL + 128, L_FA = L_GATE + 64, L_FB = L_FA + 16, L_FC = L_FB + 16, L_FD = L_FC + 16,
              L_FQ = L_FD + 16, L_TAG = L_FQ + 16, L_FO = L_TAG + 16, L_OV = L_FA + 128, L_WO = L_OV + 8 * 256, L_END = L_WO + 64 * WO_LD;
constexpr int LDS_BYTES = 108 * 1024;
static_assert(L_END <= LDS_BYTES && L_WO % 16 == 0 && L_OV % 16 == 0 && L_FA % 16 == 0, "lds");

struct Params {
  const u16* x; const u16* k; const u16* v; const u16* wq; const u16* wo;
  const u16* w1; const u16* b1; const u16* w2; const u16* b2;
  u16* out; u64* ws;
};

__device__ __forceinline__ float asf(unsigned u) { return __builtin_bit_cast(float, u); }
__device__ __forceinline__ unsigned asu(float f) { return __builtin_bit_cast(unsigned, f); }
__device__ __forceinline__ float bflo(unsigned u) { return asf(u << 16); }
__device__ __forceinline__ float bfhi(unsigned u) { return asf(u & 0xffff0000u); }
__device__ __forceinline__ unsigned f2bf(float f) { return (unsigned)__builtin_bit_cast(u16, (__bf16)f); }
__device__ __forceinline__ float rbf(float f) { return (float)(__bf16)f; }
__device__ __forceinline__ unsigned cvtpk(float lo, float hi) {
  typedef __bf16 bf2 __attribute__((ext_vector_type(2)));
  bf2 r; r.x = (__bf16)lo; r.y = (__bf16)hi;
  return __builtin_bit_cast(unsigned, r);
}
template <int CTRL> __device__ __forceinline__ float dpp(float x) {
  return __builtin_bit_cast(float, __builtin_amdgcn_mov_dpp(__builtin_bit_cast(int, x), CTRL, 0xf, 0xf, true));
}
constexpr int XOR1 = 0xB1, XOR2 = 0x4E, HMIR = 0x141, MIR = 0x140, ROR8 = 0x128, ROR4 = 0x124;
__device__ __forceinline__ float tr32(float a, float b) {
  auto r = __builtin_amdgcn_permlane32_swap(asu(a), asu(b), false, false);
  return asf(r[0]) + asf(r[1]);
}
__device__ __forceinline__ float tr16(float a, float b) {
  auto r = __builtin_amdgcn_permlane16_swap(asu(a), asu(b), false, false);
  return asf(r[0]) + asf(r[1]);
}
__device__ __forceinline__ float max_rows(float x) {
  auto s = __builtin_amdgcn_permlane16_swap(asu(x), asu(x), false, false);
  x = fmaxf(asf(s[0]), asf(s[1]));
  auto t = __builtin_amdgcn_permlane32_swap(asu(x), asu(x), false, false);
  return fmaxf(asf(t[0]), asf(t[1]));
}
__device__ __forceinline__ float sum_rows(float x) { x = tr16(x, x); return tr32(x, x); }
__device__ __forceinline__ float wave_sum(float x) {
  x += dpp<XOR1>(x); x += dpp<XOR2>(x); x += dpp<HMIR>(x); x += dpp<MIR>(x);
  return sum_rows(x);
}
__device__ __forceinline__ float treduce8(const float (&v)[8], int lane) {
  const float u0 = tr32(v[0], v[4]), u1 = tr32(v[1], v[5]), u2 = tr32(v[2], v[6]), u3 = tr32(v[3], v[7]);
  const float w0 = tr16(u0, u2), w1 = tr16(u1, u3);
  const float t0 = w0 + dpp<ROR8>(w0), t1 = w1 + dpp<ROR8>(w1);
  return (lane & 8) ? t1 : t0;
}
__device__ __forceinline__ void ln_row(const float (&v)[8], float (&o)[8]) {
  float s = 0.f;
#pragma unroll
  for (int i = 0; i < 8; ++i) s += v[i];
  s = wave_sum(s);
  const float mean = s * (1.0f / DM);
  float ss = 0.f;
#pragma unroll
  for (int i = 0; i < 8; ++i) { const float d = v[i] - mean; ss = fmaf(d, d, ss); }
  ss = wave_sum(ss);
  const float var_b = rbf(ss * (1.0f / DM)), m_b = rbf(mean);
  const float den = rbf(sqrtf(rbf(var_b + 1e-5f)));
#pragma unroll
  for (int i = 0; i < 8; ++i) o[i] = rbf(rbf(v[i] - m_b) / den);
}
#define WG_BARRIER() asm volatile("s_waitcnt lgkmcnt(0)\n\ts_barrier" ::: "memory")
#define SCHED() __builtin_amdgcn_sched_barrier(0)
#define STAMP(k) do {} while (0)

struct Tag { unsigned lo8, hi; };
__device__ __forceinline__ void gstore(u64* p, unsigned payload24, Tag t) {
  __hip_atomic_store((GAS u64*)p, ((u64)t.hi << 32) | (u64)(payload24 | t.lo8), RLX_AGENT);
}
__device__ __forceinline__ u64 gload(const u64* p) { return __hip_atomic_load((const GAS u64*)p, RLX_AGENT); }
__device__ __forceinline__ bool gok(u64 g, Tag t) {
  return (unsigned)(g >> 32) == t.hi && ((unsigned)g & 0xff000000u) == t.lo8;
}
__device__ __forceinline__ bool gok2(v4u g, Tag t) {
  return (g.y == t.hi) & (g.w == t.hi) & ((g.x & 0xff000000u) == t.lo8) & ((g.z & 0xff000000u) == t.lo8);
}
__device__ __forceinline__ unsigned enc24(float f) { unsigned u = asu(f); u += 0x7fu + ((u >> 8) & 1u); return u >> 8; }
__device__ __forceinline__ float dec24(u64 g) { return asf((unsigned)g << 8); }

__device__ __forceinline__ f32x4 mfma(v4u a, v4u b, f32x4 c) {
  return __builtin_amdgcn_mfma_f32_16x16x32_bf16(__builtin_bit_cast(bf16x8, a), __builtin_bit_cast(bf16x8, b), c, 0, 0, 0);
}

__device__ __forceinline__ float gemv16(const unsigned (&wv)[16], const float* vec, int rg, int lane) {
  float acc[8];
#pragma unroll
  for (int bb = 0; bb < 4; ++bb) {
    float a0 = 0.f, a1 = 0.f;
#pragma unroll
    for (int q4 = 0; q4 < 4; ++q4) {
      const f32x4 x0 = *(const f32x4*)(vec + bb * DM + 16 * rg + 4 * q4);
#pragma unroll
      for (int r = 0; r < 4; ++r) { a0 = fmaf(x0[r], bflo(wv[4 * q4 + r]), a0); a1 = fmaf(x0[r], bfhi(wv[4 * q4 + r]), a1); }
    }
    acc[bb * 2] = a0; acc[bb * 2 + 1] = a1;
  }
  return treduce8(acc, lane);
}
__device__ __forceinline__ void lds_set(unsigned addr, unsigned v) {
  asm volatile("s_waitcnt lgkmcnt(0)\n\tds_write_b32 %0, %1" :: "v"(addr), "v"(v) : "memory");
}
__device__ __forceinline__ void lds_wait4(unsigned addr, unsigned v) {
#pragma nounroll
  for (unsigned spin = 0; spin < (1u << 22); ++spin) {
    v4u f;
    asm volatile("ds_read_b128 %0, %1\n\ts_waitcnt lgkmcnt(0)" : "=v"(f) : "v"(addr) : "memory");
    if (__builtin_amdgcn_readfirstlane((f.x == v) & (f.y == v) & (f.z == v) & (f.w == v))) break;
    __builtin_amdgcn_s_sleep(1);
  }
}
__device__ __forceinline__ void lds_wait1(unsigned addr, unsigned v) {
#pragma nounroll
  for (unsigned spin = 0; spin < (1u << 22); ++spin) {
    unsigned f;
    asm volatile("ds_read_b32 %0, %1\n\ts_waitcnt lgkmcnt(0)" : "=v"(f) : "v"(addr) : "memory");
    if (__builtin_amdgcn_readfirstlane(f) == v) break;
    __builtin_amdgcn_s_sleep(1);
  }
}

struct Tile { v4u k[8]; v4u v[8]; };
__device__ __forceinline__ void ld8(v4u (&d)[8], const u16* base, bool valid, int off) {
  const __amdgpu_buffer_rsrc_t r = __builtin_amdgcn_make_buffer_rsrc((void*)base, 0, valid ? 8192 : 0, RSRC_W3);
#pragma unroll
  for (int i = 0; i < 8; ++i) d[i] = __builtin_amdgcn_raw_buffer_load_b128(r, off + (i & 3) * 1024, (i >> 2) * 4096, AUX_NT);
  SCHED();
}
__device__ __forceinline__ void qk_tile(v4u (&k)[8], v4u qf, float (&sc)[8], const u16* kn, bool nvalid, int koff) {
  constexpr float C2 = 0.08838834764831845f * 1.4426950408889634f;
  f32x4 d[8];
#pragma unroll
  for (int i = 0; i < 8; ++i) d[i] = mfma(k[i], qf, f32x4{0.f, 0.f, 0.f, 0.f});
  SCHED();
  ld8(k, kn, nvalid, koff);
#pragma unroll
  for (int i = 0; i < 8; ++i) sc[i] = ((dpp<0x00>(d[i][0]) + dpp<0x55>(d[i][1])) + (dpp<0xAA>(d[i][2]) + dpp<0xFF>(d[i][3]))) * C2;
}
__device__ __forceinline__ void pv_tile(v4u (&v)[8], const float (&sc)[8], float& m, float& l, f32x4 (&acc)[8], int lane,
                                        const u16* vn, bool nvalid, int voff) {
  float tm = fmaxf(fmaxf(fmaxf(sc[0], sc[1]), fmaxf(sc[2], sc[3])), fmaxf(fmaxf(sc[4], sc[5]), fmaxf(sc[6], sc[7])));
  tm = max_rows(tm);
  if (__any(tm > m)) {
    const float mn = fmaxf(m, tm), al = __builtin_amdgcn_exp2f(m - mn);
    m = mn; l *= al;
    const float ah = __shfl(al, (lane >> 4) * 4);
#pragma unroll
    for (int j = 0; j < 8; ++j) acc[j] *= ah;
  }
  v4u pk;
#pragma unroll
  for (int k = 0; k < 4; ++k) {
    const float p0 = __builtin_amdgcn_exp2f(sc[2 * k] - m), p1 = __builtin_amdgcn_exp2f(sc[2 * k + 1] - m);
    l += p0 + p1; pk[k] = cvtpk(p0, p1);
  }
#pragma unroll
  for (int jj = 0; jj < 4; ++jj) {
    v4u lo, hi;
#pragma unroll
    for (int k2 = 0; k2 < 4; ++k2) {
      lo[k2] = __builtin_amdgcn_perm(v[2 * k2 + 1][jj], v[2 * k2][jj], 0x05040100u);
      hi[k2] = __builtin_amdgcn_perm(v[2 * k2 + 1][jj], v[2 * k2][jj], 0x07060302u);
    }
    acc[2 * jj] = mfma(pk, lo, acc[2 * jj]); acc[2 * jj + 1] = mfma(pk, hi, acc[2 * jj + 1]);
  }
  SCHED();
  ld8(v, vn, nvalid, voff);
}
template <int NK> __device__ __forceinline__ void gather8(const u64* src, int nparts, Tag tag, int lane, float& s0, float& s1) {
  const __amdgpu_buffer_rsrc_t r = __builtin_amdgcn_make_buffer_rsrc((void*)src, 0, NK * 1024, RSRC_W3);
  v4u gv[NK];
#pragma nounroll
  for (unsigned spin = 0; spin < SPIN_CAP; ++spin) {
    asm volatile("" ::: "memory");
    bool ok = true;
#pragma unroll
    for (int k = 0; k < NK; ++k) gv[k] = __builtin_amdgcn_raw_buffer_load_b128(r, lane * 16 + k * 1024, 0, AUX_SC1);
#pragma unroll
    for (int k = 0; k < NK; ++k) ok = ok & (gok2(gv[k], tag) | (16 * k + (lane >> 2) >= nparts));
    if (__all(ok)) break;
    __builtin_amdgcn_s_sleep(PSLEEP);
  }
  s0 = 0.f; s1 = 0.f;
#pragma unroll
  for (int k = 0; k < NK; ++k) {
    const bool use = 16 * k + (lane >> 2) < nparts;
    s0 += use ? asf(gv[k].x << 8) : 0.f; s1 += use ? asf(gv[k].z << 8) : 0.f;
  }
  s0 += dpp<ROR4>(s0); s1 += dpp<ROR4>(s1);
  s0 += dpp<ROR8>(s0); s1 += dpp<ROR8>(s1);
  s0 = sum_rows(s0); s1 = sum_rows(s1);
}

__global__ void __launch_bounds__(NTHR, 2) fused_decode(Params p) {
  __shared__ __attribute__((aligned(16))) unsigned char lds[LDS_BYTES];
  const int tid = threadIdx.x, lane = tid & 63, w = __builtin_amdgcn_readfirstlane(tid >> 6);
  const int bid = blockIdx.x, g = bid & 7, j = bid >> 3, b = j >> 3, s = j & 7, pair = b * 8 + g, cb = g * 8 + s;
  u64* ws = p.ws;
  const unsigned lb = (unsigned)(size_t)(__attribute__((address_space(3))) unsigned char*)lds;
  STAMP(0);

  if (tid < 32) ((unsigned*)(lds + L_FA))[tid] = 0u;
  WG_BARRIER();
  const size_t kvoff = ((size_t)pair * SK + s * 2048 + w * 32) * 128;
  const u16* kb = p.k + kvoff;
  const u16* vb = p.v + kvoff;
  const int koff = ((lane & 15) >> 2) * 256 + (lane & 3) * 16 + (lane >> 4) * 64, voff = lane * 16;
  constexpr unsigned ltag = 1u;
  Tile A, B;
#define ISSUE_WINDOW(on) do { ld8(A.k, kb, on, koff); ld8(A.v, vb, on, voff); ld8(B.k, kb + 32768, on, koff); ld8(B.v, vb + 32768, on, voff); } while (0)
  if (w < 4) {
    const u64 e_raw = gload(ws + G_EPOCH);
    const v4u xr = AUX_W ? __builtin_nontemporal_load((const GAS v4u*)(p.x + w * DM + 8 * lane)) : *(const GAS v4u*)(p.x + w * DM + 8 * lane);
    const int cp = tid & 7, rg = tid >> 3;
    const bool hwg = j >= 18;
    const int f0 = 224 * g + 16 * (hwg ? j - 18 : 0);
    unsigned wqv[16], w1v[16];
    {
      const __amdgpu_buffer_rsrc_t rq = __builtin_amdgcn_make_buffer_rsrc((void*)(p.wq + g * 512 + 16 * j), 0, (ABL_RED == 2 && w != (j & 3)) ? 0 : 512 * 8192 - 8192 + 32, RSRC_W3);
#pragma unroll
      for (int r = 0; r < 16; ++r) wqv[r] = __builtin_amdgcn_raw_buffer_load_b32(rq, rg * (16 * 8192) + cp * 4, r * 8192, AUX_W);
      const __amdgpu_buffer_rsrc_t r1 = __builtin_amdgcn_make_buffer_rsrc((void*)(p.w1 + f0), 0, (hwg && !(ABL_RED == 2 && w != (j & 3))) ? 511 * FF * 2 + 32 : 0, RSRC_W3);
#pragma unroll
      for (int r = 0; r < 16; ++r) w1v[r] = __builtin_amdgcn_raw_buffer_load_b32(r1, rg * (16 * FF * 2) + cp * 4, r * FF * 2, AUX_W);
    }
    const int ob = lane >> 4, ocol = 2 * (lane & 7) + ((lane >> 3) & 1);
    const unsigned b1v = p.b1[f0 + ocol];
    SCHED();
    float* xs = (float*)(lds + L_XS);
    float* ys = (float*)(lds + L_YS);
    float* red = (float*)(lds + L_RED);
    float* redh = (float*)(lds + L_REDH);
    float xv[8], xn[8];
#pragma unroll
    for (int i = 0; i < 4; ++i) { xv[2 * i] = bflo(xr[i]); xv[2 * i + 1] = bfhi(xr[i]); }
    ln_row(xv, xn);
    *(f32x4*)(xs + w * DM + 8 * lane) = f32x4{xn[0], xn[1], xn[2], xn[3]};
    *(f32x4*)(xs + w * DM + 8 * lane + 4) = f32x4{xn[4], xn[5], xn[6], xn[7]};
    const u64 e_ = ((u64)__builtin_amdgcn_readfirstlane((unsigned)(e_raw >> 32)) << 32) | (u64)__builtin_amdgcn_readfirstlane((unsigned)e_raw);
    const u64 tag40 = ((e_ >> 24) * 0x5DEECE66DULL + 0xBULL) & ((1ULL << 40) - 1);
    Tag tag; tag.lo8 = (unsigned)(tag40 & 0xff) << 24; tag.hi = (unsigned)(tag40 >> 8);
    lds_set(lb + L_FA + 4 * w, ltag);
    STAMP(1);
    lds_wait4(lb + L_FA, ltag);
    red[w * 64 + lane] = gemv16(wqv, xs, rg, lane);
    lds_set(lb + L_FB + 4 * w, ltag);
    const __amdgpu_buffer_rsrc_t rqx = __builtin_amdgcn_make_buffer_rsrc((void*)(ws + G_QX + b * 4096 + g * 512), 0, 4096, RSRC_W3);
    v4u gq[4];
    if (w == 0) {
      lds_wait4(lb + L_FB, ltag);
      const float q = (red[lane] + red[64 + lane]) + (red[128 + lane] + red[192 + lane]);
      gstore(ws + G_QX + ob * 4096 + g * 512 + 16 * j + ocol, f2bf(q), tag);
      STAMP(2);
    }
    if (hwg) {
      float y[8];
      ln_row(xn, y);
      *(f32x4*)(ys + w * DM + 8 * lane) = f32x4{y[0], y[1], y[2], y[3]};
      *(f32x4*)(ys + w * DM + 8 * lane + 4) = f32x4{y[4], y[5], y[6], y[7]};
      lds_set(lb + L_FC + 4 * w, ltag);
      lds_wait4(lb + L_FC, ltag);
      redh[w * 64 + lane] = gemv16(w1v, ys, rg, lane);
      lds_set(lb + L_FD + 4 * w, ltag);
      if (w == 1) {
        lds_wait4(lb + L_FD, ltag);
        const float hp = (redh[lane] + redh[64 + lane]) + (redh[128 + lane] + redh[192 + lane]);
        const float pre = rbf(rbf(hp) + bflo(b1v));
        const float hv = 0.5f * pre * (1.0f + tanhf(0.7978845608028654f * (pre + 0.044715f * pre * pre * pre)));
        gstore(ws + G_HX + ob * 2048 + f0 + ocol, f2bf(hv), tag);
      }
    }
    if (w == 0) {
#pragma nounroll
      for (unsigned spin = 0; spin < SPIN_CAP; ++spin) {
        asm volatile("" ::: "memory");
        bool ok = true;
#pragma unroll
        for (int k = 0; k < 4; ++k) gq[k] = __builtin_amdgcn_raw_buffer_load_b128(rqx, lane * 16 + k * 1024, 0, AUX_G);
#pragma unroll
        for (int k = 0; k < 4; ++k) ok = ok & gok2(gq[k], tag);
        if (__all(ok)) break;
        __builtin_amdgcn_s_sleep(1);
      }
#pragma unroll
      for (int k = 0; k < 4; ++k) ((unsigned*)(lds + L_QS))[64 * k + lane] = (gq[k].x & 0xffffu) | (gq[k].z << 16);
      *(v2u*)(lds + L_TAG) = v2u{tag.lo8, tag.hi};
      lds_set(lb + L_FQ, ltag);
      STAMP(3);
    }
  }
  SCHED();
  ISSUE_WINDOW(true);
  lds_wait1(lb + L_FQ, ltag);
  Tag tag;
  { const v2u t = *(const v2u*)(lds + L_TAG); tag.lo8 = __builtin_amdgcn_readfirstlane(t.x); tag.hi = __builtin_amdgcn_readfirstlane(t.y); }
  STAMP(4);
  const v4u qf = *(const v4u*)(lds + L_QS + ((lane & 15) >> 2) * 256 + ((lane & 3) + 4 * (lane >> 4)) * 16);

  float m = -INFINITY, lsum = 0.f;
  f32x4 acc[8];
#pragma unroll
  for (int i = 0; i < 8; ++i) acc[i] = f32x4{0.f, 0.f, 0.f, 0.f};
  float sA[8], sB[8];
#pragma nounroll
  for (int it = 0; it < 4; ++it) {
    const bool nv = it < 3;
    const size_t o = (size_t)(nv ? 2 * it + 2 : 0) * 32768;
    qk_tile(A.k, qf, sA, kb + o, nv, koff);
    pv_tile(A.v, sA, m, lsum, acc, lane, vb + o, nv, voff);
    qk_tile(B.k, qf, sB, kb + o + 32768, nv, koff);
    pv_tile(B.v, sB, m, lsum, acc, lane, vb + o + 32768, nv, voff);
  }
  lsum = sum_rows(lsum);
  STAMP(5);
  float* om = (float*)(lds + L_OM);
  float* sm = (float*)(lds + L_SM);
  float* sl = (float*)(lds + L_SL);
  *(f32x4*)(om + w * 512 + lane * 8) = f32x4{acc[0][0], acc[1][0], acc[2][0], acc[3][0]};
  *(f32x4*)(om + w * 512 + lane * 8 + 4) = f32x4{acc[4][0], acc[5][0], acc[6][0], acc[7][0]};
  if (lane < 16 && (lane & 3) == 0) { sm[w * 4 + (lane >> 2)] = m; sl[w * 4 + (lane >> 2)] = lsum; }
  const int r8 = lane >> 3, c8 = lane & 7;

  if (w < 4) {
    v4u t16[16];
    const __amdgpu_buffer_rsrc_t rwo = __builtin_amdgcn_make_buffer_rsrc((void*)(p.wo + (size_t)(g * 512 + 64 * s + 16 * w) * DM), 0, (ABL_RED && w != b) ? 0 : 16384, RSRC_W3);
#pragma unroll
    for (int r = 0; r < 16; ++r) t16[r] = __builtin_amdgcn_raw_buffer_load_b128(rwo, voff + (r & 3) * 1024, (r >> 2) * 4096, AUX_W);
    v4u w2v[8];
    const int f0 = 256 * g + 64 * w;
    const __amdgpu_buffer_rsrc_t rw2 = __builtin_amdgcn_make_buffer_rsrc((void*)(p.w2 + (size_t)(g < 7 ? f0 : 0) * DM + 64 * s), 0, (g < 7 && !(ABL_RED && w != b)) ? 63 * 1024 + 128 : 0, RSRC_W3);
#pragma unroll
    for (int i = 0; i < 8; ++i) w2v[i] = __builtin_amdgcn_raw_buffer_load_b128(rw2, r8 * 1024 + c8 * 16, i * 8192, AUX_W);
    const u64* hsrc = ws + G_HX + b * 2048 + f0 + lane;
    u64 hg = gload(hsrc);
    SCHED();
#pragma unroll
    for (int r = 0; r < 16; ++r) *(v4u*)(lds + L_WO + (16 * w + r) * WO_LD + voff) = t16[r];
    if (g < 7) {
      if (!__all(gok(hg, tag))) {
        unsigned spin = 0;
        do { __builtin_amdgcn_s_sleep(PSLEEP); hg = gload(hsrc); } while (!__all(gok(hg, tag)) && ++spin < SPIN_CAP);
      }
      const float hl = bflo((unsigned)hg);
      float ma[8];
#pragma unroll
      for (int e = 0; e < 8; ++e) ma[e] = 0.f;
#pragma unroll
      for (int i = 0; i < 8; ++i) {
        const float hv = __shfl(hl, 8 * i + r8);
#pragma unroll
        for (int e = 0; e < 4; ++e) { ma[2 * e] = fmaf(hv, bflo(w2v[i][e]), ma[2 * e]); ma[2 * e + 1] = fmaf(hv, bfhi(w2v[i][e]), ma[2 * e + 1]); }
      }
      const float r = treduce8(ma, lane);
      gstore(ws + G_MX + ((size_t)(b * 64 + 8 * s + c8) * 32 + 4 * g + w) * 8 + r8, enc24(r), tag);
    }
    if (w == 1) {
      const unsigned b2p = *(const GAS unsigned*)(p.b2 + cb * 8 + 2 * (lane & 3));
      float m0, m1;
      gather8<2>(ws + G_MX + (size_t)(b * 64 + cb) * 256, 28, tag, lane, m0, m1);
      m0 = rbf(rbf(m0) + bflo(b2p)); m1 = rbf(rbf(m1) + bfhi(b2p));
      const float g0 = rbf(m0 / (1.0f + __expf(-m0))), g1 = rbf(m1 / (1.0f + __expf(-m1)));
      if (lane < 4) { ((float*)(lds + L_GATE))[2 * lane] = g0; ((float*)(lds + L_GATE))[2 * lane + 1] = g1; }
    }
  }
  STAMP(6);
#pragma nounroll
  for (int pass = (DRY && w == 2) ? 0 : 1; pass < 2; ++pass) {
  int real_v = pass;
  asm volatile("" : "+v"(real_v));
  const int real = __builtin_amdgcn_readfirstlane(real_v);
  u64* wsx = real ? ws : ws + G_SHADOW;
  u16* outx = real ? p.out : (u16*)(ws + G_SHADOW + 64);
  if (real) WG_BARRIER();
  {
    const int h = tid >> 7;
    float M = sm[h];
#pragma unroll
    for (int ww = 1; ww < 8; ++ww) M = fmaxf(M, sm[ww * 4 + h]);
    float O = 0.f, L = 0.f;
#pragma unroll
    for (int ww = 0; ww < 8; ++ww) {
      const float f = __builtin_amdgcn_exp2f(sm[ww * 4 + h] - M);
      O = fmaf(f, om[ww * 512 + tid], O); L = fmaf(f, sl[ww * 4 + h], L);
    }
    gstore(wsx + G_PX + (size_t)(pair * 8 + s) * 512 + tid, enc24(O), tag);
    const int d = tid & 127;
    if (d < 4) {
      const unsigned u = (d & 2) ? asu(L) : asu(M);
      gstore(wsx + G_PS + (size_t)((pair * 4 + h) * 8 + s) * 4 + d, (d & 1) ? (u & 0xffffu) : (u >> 16), tag);
    }
  }
  STAMP(7);
  {
    const bool pw = (w == PW) || !real;
    const __amdgpu_buffer_rsrc_t rp = __builtin_amdgcn_make_buffer_rsrc((void*)(wsx + G_PX + (size_t)pair * 4096 + 64 * s), 0, pw ? 8 * 4096 : 0, RSRC_W3);
    const __amdgpu_buffer_rsrc_t rs = __builtin_amdgcn_make_buffer_rsrc((void*)(wsx + G_PS + (size_t)(pair * 4 + (s >> 1)) * 32), 0, pw ? 256 : 0, RSRC_W3);
    const int poff = (lane >> 5) * 4096 + (lane & 31) * 16;
    v4u gp[4]; v2u gs;
#define PLOAD() do { _Pragma("unroll") for (int k = 0; k < 4; ++k) gp[k] = __builtin_amdgcn_raw_buffer_load_b128(rp, poff, k * 8192, AUX_SC1); \
      gs = __builtin_amdgcn_raw_buffer_load_b64(rs, (lane & 31) * 8, 0, AUX_SC1); } while (0)
    PLOAD();
    SCHED();
    float wf[8][8];
#pragma unroll
    for (int i = 0; i < 8; ++i) {
      const v4u t = *(const v4u*)(lds + L_WO + (8 * r8 + i) * WO_LD + w * 128 + c8 * 16);
#pragma unroll
      for (int e = 0; e < 4; ++e) { wf[i][2 * e] = bflo(t[e]); wf[i][2 * e + 1] = bfhi(t[e]); }
    }
#pragma unroll
    for (int i = 0; i < 8; ++i)
#pragma unroll
      for (int e = 0; e < 8; ++e) asm volatile("" : "+v"(wf[i][e]));
    SCHED();
    float* ov = (float*)(lds + L_OV + (real ? 0 : 256));
    if (pw) {
#define POK() __all(gok2(gp[0], tag) & gok2(gp[1], tag) & gok2(gp[2], tag) & gok2(gp[3], tag) & (gs.y == tag.hi) & ((gs.x & 0xff000000u) == tag.lo8))
      if (real && !POK()) {
        unsigned spin = 0;
        do {
          __builtin_amdgcn_s_sleep(PSLEEP);
          asm volatile("" ::: "memory");
          PLOAD();
        } while (!POK() && ++spin < SPIN_CAP);
      }
      STAMP(8);
      const unsigned sv = gs.x & 0xffffu;
      const float Mk = asf((asu(dpp<0x00>(asf(sv))) << 16) | asu(dpp<0x55>(asf(sv))));
      const float Lk = asf((asu(dpp<0xAA>(asf(sv))) << 16) | asu(dpp<0xFF>(asf(sv))));
      float M = fmaxf(Mk, dpp<ROR4>(Mk)); M = fmaxf(M, dpp<ROR8>(M));
      { auto t = __builtin_amdgcn_permlane16_swap(asu(M), asu(M), false, false); M = fmaxf(asf(t[0]), asf(t[1])); }
      const float fk = __builtin_amdgcn_exp2f(Mk - M);
      float L = fk * Lk; L += dpp<ROR4>(L); L += dpp<ROR8>(L); L = tr16(L, L);
      float o0 = 0.f, o1 = 0.f;
#pragma unroll
      for (int k = 0; k < 4; ++k) {
        const float fe = asf(__builtin_amdgcn_readlane(asu(fk), 8 * k)), fo = asf(__builtin_amdgcn_readlane(asu(fk), 8 * k + 4));
        const float f = (lane >> 5) ? fo : fe;
        o0 = fmaf(f, asf(gp[k].x << 8), o0); o1 = fmaf(f, asf(gp[k].z << 8), o1);
      }
      const float ot = tr32(o0, o1);
      ov[2 * (lane & 31) + (lane >> 5)] = rbf(ot / L);
      lds_set(lb + L_FO, real ? ltag : 0u);
    } else {
      lds_wait1(lb + L_FO, ltag);
    }
    asm volatile("s_waitcnt lgkmcnt(0)" ::: "memory");
    const f32x4 oa = *(const f32x4*)(ov + 8 * r8), ob4 = *(const f32x4*)(ov + 8 * r8 + 4);
    float sa[8];
#pragma unroll
    for (int e = 0; e < 8; ++e) {
      float t = oa[0] * wf[0][e];
      t = fmaf(oa[1], wf[1][e], t); t = fmaf(oa[2], wf[2][e], t); t = fmaf(oa[3], wf[3][e], t);
      t = fmaf(ob4[0], wf[4][e], t); t = fmaf(ob4[1], wf[5][e], t); t = fmaf(ob4[2], wf[6][e], t); t = fmaf(ob4[3], wf[7][e], t);
      sa[e] = t;
    }
    const float sp = treduce8(sa, lane);
    gstore(wsx + G_SX + ((size_t)(b * 64 + 8 * w + c8) * 64 + cb) * 8 + r8, enc24(sp), tag);
  }
  STAMP(9);
  if (w == 0 || !real) {
    const unsigned xp = *(const GAS unsigned*)(p.x + b * DM + cb * 8 + 2 * (lane & 3));
    const float2 gt = *(const float2*)(lds + L_GATE + 8 * (lane & 3));
    float s0, s1;
    gather8<4>(wsx + G_SX + (size_t)(b * 64 + cb) * 512, real ? 64 : 0, tag, lane, s0, s1);
    STAMP(10);
    const float o0 = bflo(xp) + rbf(rbf(s0) * gt.x), o1 = bfhi(xp) + rbf(rbf(s1) * gt.y);
    if (lane < 4) *(GAS unsigned*)(outx + b * DM + cb * 8 + 2 * lane) = cvtpk(o0, o1);
    if (bid == 0 && lane == 0) __hip_atomic_store((GAS u64*)(wsx + G_EPOCH), ((u64)tag.hi << 32) | tag.lo8, RLX_AGENT);
  }
  }
}

}

extern "C" void kernel_launch(void* const* d_in, const int* in_sizes, int n_in,
                              void* d_out, int out_size, void* d_ws, size_t ws_size,
                              hipStream_t stream) {
  Params p;
  p.x = (const u16*)d_in[0]; p.k = (const u16*)d_in[1]; p.v = (const u16*)d_in[2];
  p.wq = (const u16*)d_in[3]; p.wo = (const u16*)d_in[4];
  p.w1 = (const u16*)d_in[6]; p.b1 = (const u16*)d_in[7]; p.w2 = (const u16*)d_in[8]; p.b2 = (const u16*)d_in[9];
  p.out = (u16*)d_out; p.ws = (u64*)d_ws;
  hipLaunchKernelGGL(fused_decode, dim3(NWG), dim3(NTHR), 0, stream, p);
}
```

```cpp
#define STAMPS
#include <cstdio>
#include <hip/hip_runtime.h>
#include <hip/hip_bf16.h>
#include <stdint.h>

namespace {

typedef unsigned v4u __attribute__((ext_vector_type(4)));
typedef unsigned v2u __attribute__((ext_vector_type(2)));
typedef float f32x4 __attribute__((ext_vector_type(4)));
typedef short bf16x8 __attribute__((ext_vector_type(8)));
typedef unsigned long long u64;
typedef unsigned short u16;
#define GAS __attribute__((address_space(1)))
#define RLX_AGENT __ATOMIC_RELAXED, __HIP_MEMORY_SCOPE_AGENT

constexpr int DM = 512, SK = 16384, FF = 1792;
constexpr int NWG = 256, NTHR = 512;
constexpr int W = 32;
constexpr int RSRC_W3 = 0x00020000;
constexpr int AUX_NT = 2, AUX_SC1 = 16;
#define AUX_W 0
#define AUX_K 2
#define AUX_V 2
#define TOPWIN 0
#define PW 1
#define ABL_RED 0
#define DRY 0
#define AUX_G 16
#define PSLEEP 2
constexpr unsigned SPIN_CAP = 1u << 16;

constexpr size_t G_EPOCH = 0;
constexpr size_t G_QX = 512;
constexpr size_t G_PS = G_QX + 4 * 4096;
constexpr size_t G_PX = G_PS + 32 * 8 * 16;
constexpr size_t G_SX = G_PX + 32 * 8 * 512;
constexpr size_t G_HX = G_SX + 4 * 64 * 64 * 8;
constexpr size_t G_MX = G_HX + 4 * 2048;
constexpr size_t G_END = G_MX + 4 * 64 * 32 * 8;
constexpr size_t G_SHADOW = 524288;
static_assert(G_END <= G_SHADOW, "ws");

constexpr int WO_LD = 1040;
constexpr int L_OM = 0;
constexpr int L_XS = 16384, L_YS = L_XS + 8192;
constexpr int L_RED = 32768, L_REDH = L_RED + 1024, L_QS = L_REDH + 1024, L_SM = L_QS + 1024, L_SL = L_SM + 128,
              L_GATE = L_SL + 128, L_FA = L_GATE + 64, L_FB = L_FA + 16, L_FC = L_FB + 16, L_FD = L_FC + 16,
              L_FQ = L_FD + 16, L_TAG = L_FQ + 16, L_FO = L_TAG + 16, L_OV = L_FA + 128, L_WO = L_OV + 8 * 256, L_END = L_WO + 64 * WO_LD;
constexpr int LDS_BYTES = 108 * 1024;
static_assert(L_END <= LDS_BYTES && L_WO % 16 == 0 && L_OV % 16 == 0 && L_FA % 16 == 0, "lds");

struct Params {
  const u16* x; const u16* k; const u16* v; const u16* wq; const u16* wo;
  const u16* w1; const u16* b1; const u16* w2; const u16* b2;
  u16* out; u64* ws;
};

__device__ __forceinline__ float asf(unsigned u) { return __builtin_bit_cast(float, u); }
__device__ __forceinline__ unsigned asu(float f) { return __builtin_bit_cast(unsigned, f); }
__device__ __forceinline__ float bflo(unsigned u) { return asf(u << 16); }
__device__ __forceinline__ float bfhi(unsigned u) { return asf(u & 0xffff0000u); }
__device__ __forceinline__ unsigned f2bf(float f) { return (unsigned)__builtin_bit_cast(u16, (__bf16)f); }
__device__ __forceinline__ float rbf(float f) { return (float)(__bf16)f; }
__device__ __forceinline__ unsigned cvtpk(float lo, float hi) {
  typedef __bf16 bf2 __attribute__((ext_vector_type(2)));
  bf2 r; r.x = (__bf16)lo; r.y = (__bf16)hi;
  return __builtin_bit_cast(unsigned, r);
}
template <int CTRL> __device__ __forceinline__ float dpp(float x) {
  return __builtin_bit_cast(float, __builtin_amdgcn_mov_dpp(__builtin_bit_cast(int, x), CTRL, 0xf, 0xf, true));
}
constexpr int XOR1 = 0xB1, XOR2 = 0x4E, HMIR = 0x141, MIR = 0x140, ROR8 = 0x128, ROR4 = 0x124;
__device__ __forceinline__ float tr32(float a, float b) {
  auto r = __builtin_amdgcn_permlane32_swap(asu(a), asu(b), false, false);
  return asf(r[0]) + asf(r[1]);
}
__device__ __forceinline__ float tr16(float a, float b) {
  auto r = __builtin_amdgcn_permlane16_swap(asu(a), asu(b), false, false);
  return asf(r[0]) + asf(r[1]);
}
__device__ __forceinline__ float max_rows(float x) {
  auto s = __builtin_amdgcn_permlane16_swap(asu(x), asu(x), false, false);
  x = fmaxf(asf(s[0]), asf(s[1]));
  auto t = __builtin_amdgcn_permlane32_swap(asu(x), asu(x), false, false);
  return fmaxf(asf(t[0]), asf(t[1]));
}
__device__ __forceinline__ float sum_rows(float x) { x = tr16(x, x); return tr32(x, x); }
__device__ __forceinline__ float wave_sum(float x) {
  x += dpp<XOR1>(x); x += dpp<XOR2>(x); x += dpp<HMIR>(x); x += dpp<MIR>(x);
  return sum_rows(x);
}
__device__ __forceinline__ float treduce8(const float (&v)[8], int lane) {
  const float u0 = tr32(v[0], v[4]), u1 = tr32(v[1], v[5]), u2 = tr32(v[2], v[6]), u3 = tr32(v[3], v[7]);
  const float w0 = tr16(u0, u2), w1 = tr16(u1, u3);
  const float t0 = w0 + dpp<ROR8>(w0), t1 = w1 + dpp<ROR8>(w1);
  return (lane & 8) ? t1 : t0;
}
__device__ __forceinline__ void ln_row(const float (&v)[8], float (&o)[8]) {
  float s = 0.f;
#pragma unroll
  for (int i = 0; i < 8; ++i) s += v[i];
  s = wave_sum(s);
  const float mean = s * (1.0f / DM);
  float ss = 0.f;
#pragma unroll
  for (int i = 0; i < 8; ++i) { const float d = v[i] - mean; ss = fmaf(d, d, ss); }
  ss = wave_sum(ss);
  const float var_b = rbf(ss * (1.0f / DM)), m_b = rbf(mean);
  const float den = rbf(sqrtf(rbf(var_b + 1e-5f)));
#pragma unroll
  for (int i = 0; i < 8; ++i) o[i] = rbf(rbf(v[i] - m_b) / den);
}
#define WG_BARRIER() asm volatile("s_waitcnt lgkmcnt(0)\n\ts_barrier" ::: "memory")
#define SCHED() __builtin_amdgcn_sched_barrier(0)
#define NSTAMP 12
#define STAMP_OFF (8u << 20)
#define STAMP(k) do { SCHED(); stamp[k] = __builtin_amdgcn_s_memrealtime(); SCHED(); } while (0)

struct Tag { unsigned lo8, hi; };
__device__ __forceinline__ void gstore(u64* p, unsigned payload24, Tag t) {
  __hip_atomic_store((GAS u64*)p, ((u64)t.hi << 32) | (u64)(payload24 | t.lo8), RLX_AGENT);
}
__device__ __forceinline__ u64 gload(const u64* p) { return __hip_atomic_load((const GAS u64*)p, RLX_AGENT); }
__device__ __forceinline__ bool gok(u64 g, Tag t) {
  return (unsigned)(g >> 32) == t.hi && ((unsigned)g & 0xff000000u) == t.lo8;
}
__device__ __forceinline__ bool gok2(v4u g, Tag t) {
  return (g.y == t.hi) & (g.w == t.hi) & ((g.x & 0xff000000u) == t.lo8) & ((g.z & 0xff000000u) == t.lo8);
}
__device__ __forceinline__ unsigned enc24(float f) { unsigned u = asu(f); u += 0x7fu + ((u >> 8) & 1u); return u >> 8; }
__device__ __forceinline__ float dec24(u64 g) { return asf((unsigned)g << 8); }

__device__ __forceinline__ f32x4 mfma(v4u a, v4u b, f32x4 c) {
  return __builtin_amdgcn_mfma_f32_16x16x32_bf16(__builtin_bit_cast(bf16x8, a), __builtin_bit_cast(bf16x8, b), c, 0, 0, 0);
}

__device__ __forceinline__ float gemv16(const unsigned (&wv)[16], const float* vec, int rg, int lane) {
  float acc[8];
#pragma unroll
  for (int bb = 0; bb < 4; ++bb) {
    float a0 = 0.f, a1 = 0.f;
#pragma unroll
    for (int q4 = 0; q4 < 4; ++q4) {
      const f32x4 x0 = *(const f32x4*)(vec + bb * DM + 16 * rg + 4 * q4);
#pragma unroll
      for (int r = 0; r < 4; ++r) { a0 = fmaf(x0[r], bflo(wv[4 * q4 + r]), a0); a1 = fmaf(x0[r], bfhi(wv[4 * q4 + r]), a1); }
    }
    acc[bb * 2] = a0; acc[bb * 2 + 1] = a1;
  }
  return treduce8(acc, lane);
}
__device__ __forceinline__ void lds_set(unsigned addr, unsigned v) {
  asm volatile("s_waitcnt lgkmcnt(0)\n\tds_write_b32 %0, %1" :: "v"(addr), "v"(v) : "memory");
}
__device__ __forceinline__ void lds_wait4(unsigned addr, unsigned v) {
#pragma nounroll
  for (unsigned spin = 0; spin < (1u << 22); ++spin) {
    v4u f;
    asm volatile("ds_read_b128 %0, %1\n\ts_waitcnt lgkmcnt(0)" : "=v"(f) : "v"(addr) : "memory");
    if (__builtin_amdgcn_readfirstlane((f.x == v) & (f.y == v) & (f.z == v) & (f.w == v))) break;
    __builtin_amdgcn_s_sleep(1);
  }
}
__device__ __forceinline__ void lds_wait1(unsigned addr, unsigned v) {
#pragma nounroll
  for (unsigned spin = 0; spin < (1u << 22); ++spin) {
    unsigned f;
    asm volatile("ds_read_b32 %0, %1\n\ts_waitcnt lgkmcnt(0)" : "=v"(f) : "v"(addr) : "memory");
    if (__builtin_amdgcn_readfirstlane(f) == v) break;
    __builtin_amdgcn_s_sleep(1);
  }
}

struct Tile { v4u k[8]; v4u v[8]; };
__device__ __forceinline__ void ld8(v4u (&d)[8], const u16* base, bool valid, int off) {
  const __amdgpu_buffer_rsrc_t r = __builtin_amdgcn_make_buffer_rsrc((void*)base, 0, valid ? 8192 : 0, RSRC_W3);
#pragma unroll
  for (int i = 0; i < 8; ++i) d[i] = __builtin_amdgcn_raw_buffer_load_b128(r, off + (i & 3) * 1024, (i >> 2) * 4096, AUX_NT);
  SCHED();
}
__device__ __forceinline__ void qk_tile(v4u (&k)[8], v4u qf, float (&sc)[8], const u16* kn, bool nvalid, int koff) {
  constexpr float C2 = 0.08838834764831845f * 1.4426950408889634f;
  f32x4 d[8];
#pragma unroll
  for (int i = 0; i < 8; ++i) d[i] = mfma(k[i], qf, f32x4{0.f, 0.f, 0.f, 0.f});
  SCHED();
  ld8(k, kn, nvalid, koff);
#pragma unroll
  for (int i = 0; i < 8; ++i) sc[i] = ((dpp<0x00>(d[i][0]) + dpp<0x55>(d[i][1])) + (dpp<0xAA>(d[i][2]) + dpp<0xFF>(d[i][3]))) * C2;
}
__device__ __forceinline__ void pv_tile(v4u (&v)[8], const float (&sc)[8], float& m, float& l, f32x4 (&acc)[8], int lane,
                                        const u16* vn, bool nvalid, int voff) {
  float tm = fmaxf(fmaxf(fmaxf(sc[0], sc[1]), fmaxf(sc[2], sc[3])), fmaxf(fmaxf(sc[4], sc[5]), fmaxf(sc[6], sc[7])));
  tm = max_rows(tm);
  if (__any(tm > m)) {
    const float mn = fmaxf(m, tm), al = __builtin_amdgcn_exp2f(m - mn);
    m = mn; l *= al;
    const float ah = __shfl(al, (lane >> 4) * 4);
#pragma unroll
    for (int j = 0; j < 8; ++j) acc[j] *= ah;
  }
  v4u pk;
#pragma unroll
  for (int k = 0; k < 4; ++k) {
    const float p0 = __builtin_amdgcn_exp2f(sc[2 * k] - m), p1 = __builtin_amdgcn_exp2f(sc[2 * k + 1] - m);
    l += p0 + p1; pk[k] = cvtpk(p0, p1);
  }
#pragma unroll
  for (int jj = 0; jj < 4; ++jj) {
    v4u lo, hi;
#pragma unroll
    for (int k2 = 0; k2 < 4; ++k2) {
      lo[k2] = __builtin_amdgcn_perm(v[2 * k2 + 1][jj], v[2 * k2][jj], 0x05040100u);
      hi[k2] = __builtin_amdgcn_perm(v[2 * k2 + 1][jj], v[2 * k2][jj], 0x07060302u);
    }
    acc[2 * jj] = mfma(pk, lo, acc[2 * jj]); acc[2 * jj + 1] = mfma(pk, hi, acc[2 * jj + 1]);
  }
  SCHED();
  ld8(v, vn, nvalid, voff);
}
template <int NK> __device__ __forceinline__ void gather8(const u64* src, int nparts, Tag tag, int lane, float& s0, float& s1) {
  const __amdgpu_buffer_rsrc_t r = __builtin_amdgcn_make_buffer_rsrc((void*)src, 0, NK * 1024, RSRC_W3);
  v4u gv[NK];
#pragma nounroll
  for (unsigned spin = 0; spin < SPIN_CAP; ++spin) {
    asm volatile("" ::: "memory");
    bool ok = true;
#pragma unroll
    for (int k = 0; k < NK; ++k) gv[k] = __builtin_amdgcn_raw_buffer_load_b128(r, lane * 16 + k * 1024, 0, AUX_SC1);
#pragma unroll
    for (int k = 0; k < NK; ++k) ok = ok & (gok2(gv[k], tag) | (16 * k + (lane >> 2) >= nparts));
    if (__all(ok)) break;
    __builtin_amdgcn_s_sleep(PSLEEP);
  }
  s0 = 0.f; s1 = 0.f;
#pragma unroll
  for (int k = 0; k < NK; ++k) {
    const bool use = 16 * k + (lane >> 2) < nparts;
    s0 += use ? asf(gv[k].x << 8) : 0.f; s1 += use ? asf(gv[k].z << 8) : 0.f;
  }
  s0 += dpp<ROR4>(s0); s1 += dpp<ROR4>(s1);
  s0 += dpp<ROR8>(s0); s1 += dpp<ROR8>(s1);
  s0 = sum_rows(s0); s1 = sum_rows(s1);
}

__global__ void __launch_bounds__(NTHR, 2) fused_decode(Params p) {
  __shared__ __attribute__((aligned(16))) unsigned char lds[LDS_BYTES];
  const int tid = threadIdx.x, lane = tid & 63, w = __builtin_amdgcn_readfirstlane(tid >> 6);
  const int bid = blockIdx.x, g = bid & 7, j = bid >> 3, b = j >> 3, s = j & 7, pair = b * 8 + g, cb = g * 8 + s;
  u64* ws = p.ws;
  const unsigned lb = (unsigned)(size_t)(__attribute__((address_space(3))) unsigned char*)lds;
  u64 stamp[NSTAMP];
  for (int k = 0; k < NSTAMP; ++k) stamp[k] = 0;
  STAMP(0);

  if (tid < 32) ((unsigned*)(lds + L_FA))[tid] = 0u;
  WG_BARRIER();
  const size_t kvoff = ((size_t)pair * SK + s * 2048 + w * 32) * 128;
  const u16* kb = p.k + kvoff;
  const u16* vb = p.v + kvoff;
  const int koff = ((lane & 15) >> 2) * 256 + (lane & 3) * 16 + (lane >> 4) * 64, voff = lane * 16;
  constexpr unsigned ltag = 1u;
  Tile A, B;
#define ISSUE_WINDOW(on) do { ld8(A.k, kb, on, koff); ld8(A.v, vb, on, voff); ld8(B.k, kb + 32768, on, koff); ld8(B.v, vb + 32768, on, voff); } while (0)
  if (w < 4) {
    const u64 e_raw = gload(ws + G_EPOCH);
    const v4u xr = AUX_W ? __builtin_nontemporal_load((const GAS v4u*)(p.x + w * DM + 8 * lane)) : *(const GAS v4u*)(p.x + w * DM + 8 * lane);
    const int cp = tid & 7, rg = tid >> 3;
    const bool hwg = j >= 18;
    const int f0 = 224 * g + 16 * (hwg ? j - 18 : 0);
    unsigned wqv[16], w1v[16];
    {
      const __amdgpu_buffer_rsrc_t rq = __builtin_amdgcn_make_buffer_rsrc((void*)(p.wq + g * 512 + 16 * j), 0, (ABL_RED == 2 && w != (j & 3)) ? 0 : 512 * 8192 - 8192 + 32, RSRC_W3);
#pragma unroll
      for (int r = 0; r < 16; ++r) wqv[r] = __builtin_amdgcn_raw_buffer_load_b32(rq, rg * (16 * 8192) + cp * 4, r * 8192, AUX_W);
      const __amdgpu_buffer_rsrc_t r1 = __builtin_amdgcn_make_buffer_rsrc((void*)(p.w1 + f0), 0, (hwg && !(ABL_RED == 2 && w != (j & 3))) ? 511 * FF * 2 + 32 : 0, RSRC_W3);
#pragma unroll
      for (int r = 0; r < 16; ++r) w1v[r] = __builtin_amdgcn_raw_buffer_load_b32(r1, rg * (16 * FF * 2) + cp * 4, r * FF * 2, AUX_W);
    }
    const int ob = lane >> 4, ocol = 2 * (lane & 7) + ((lane >> 3) & 1);
    const unsigned b1v = p.b1[f0 + ocol];
    SCHED();
    float* xs = (float*)(lds + L_XS);
    float* ys = (float*)(lds + L_YS);
    float* red = (float*)(lds + L_RED);
    float* redh = (float*)(lds + L_REDH);
    float xv[8], xn[8];
#pragma unroll
    for (int i = 0; i < 4; ++i) { xv[2 * i] = bflo(xr[i]); xv[2 * i + 1] = bfhi(xr[i]); }
    ln_row(xv, xn);
    *(f32x4*)(xs + w * DM + 8 * lane) = f32x4{xn[0], xn[1], xn[2], xn[3]};
    *(f32x4*)(xs + w * DM + 8 * lane + 4) = f32x4{xn[4], xn[5], xn[6], xn[7]};
    const u64 e_ = ((u64)__builtin_amdgcn_readfirstlane((unsigned)(e_raw >> 32)) << 32) | (u64)__builtin_amdgcn_readfirstlane((unsigned)e_raw);
    const u64 tag40 = ((e_ >> 24) * 0x5DEECE66DULL + 0xBULL) & ((1ULL << 40) - 1);
    Tag tag; tag.lo8 = (unsigned)(tag40 & 0xff) << 24; tag.hi = (unsigned)(tag40 >> 8);
    lds_set(lb + L_FA + 4 * w, ltag);
    STAMP(1);
    lds_wait4(lb + L_FA, ltag);
    red[w * 64 + lane] = gemv16(wqv, xs, rg, lane);
    lds_set(lb + L_FB + 4 * w, ltag);
    const __amdgpu_buffer_rsrc_t rqx = __builtin_amdgcn_make_buffer_rsrc((void*)(ws + G_QX + b * 4096 + g * 512), 0, 4096, RSRC_W3);
    v4u gq[4];
    if (w == 0) {
      lds_wait4(lb + L_FB, ltag);
      const float q = (red[lane] + red[64 + lane]) + (red[128 + lane] + red[192 + lane]);
      gstore(ws + G_QX + ob * 4096 + g * 512 + 16 * j + ocol, f2bf(q), tag);
      STAMP(2);
    }
    if (hwg) {
      float y[8];
      ln_row(xn, y);
      *(f32x4*)(ys + w * DM + 8 * lane) = f32x4{y[0], y[1], y[2], y[3]};
      *(f32x4*)(ys + w * DM + 8 * lane + 4) = f32x4{y[4], y[5], y[6], y[7]};
      lds_set(lb + L_FC + 4 * w, ltag);
      lds_wait4(lb + L_FC, ltag);
      redh[w * 64 + lane] = gemv16(w1v, ys, rg, lane);
      lds_set(lb + L_FD + 4 * w, ltag);
      if (w == 1) {
        lds_wait4(lb + L_FD, ltag);
        const float hp = (redh[lane] + redh[64 + lane]) + (redh[128 + lane] + redh[192 + lane]);
        const float pre = rbf(rbf(hp) + bflo(b1v));
        const float hv = 0.5f * pre * (1.0f + tanhf(0.7978845608028654f * (pre + 0.044715f * pre * pre * pre)));
        gstore(ws + G_HX + ob * 2048 + f0 + ocol, f2bf(hv), tag);
      }
    }
    if (w == 0) {
#pragma nounroll
      for (unsigned spin = 0; spin < SPIN_CAP; ++spin) {
        asm volatile("" ::: "memory");
        bool ok = true;
#pragma unroll
        for (int k = 0; k < 4; ++k) gq[k] = __builtin_amdgcn_raw_buffer_load_b128(rqx, lane * 16 + k * 1024, 0, AUX_G);
#pragma unroll
        for (int k = 0; k < 4; ++k) ok = ok & gok2(gq[k], tag);
        if (__all(ok)) break;
        __builtin_amdgcn_s_sleep(1);
      }
#pragma unroll
      for (int k = 0; k < 4; ++k) ((unsigned*)(lds + L_QS))[64 * k + lane] = (gq[k].x & 0xffffu) | (gq[k].z << 16);
      *(v2u*)(lds + L_TAG) = v2u{tag.lo8, tag.hi};
      lds_set(lb + L_FQ, ltag);
      STAMP(3);
    }
  }
  SCHED();
  ISSUE_WINDOW(true);
  lds_wait1(lb + L_FQ, ltag);
  Tag tag;
  { const v2u t = *(const v2u*)(lds + L_TAG); tag.lo8 = __builtin_amdgcn_readfirstlane(t.x); tag.hi = __builtin_amdgcn_readfirstlane(t.y); }
  STAMP(4);
  const v4u qf = *(const v4u*)(lds + L_QS + ((lane & 15) >> 2) * 256 + ((lane & 3) + 4 * (lane >> 4)) * 16);

  float m = -INFINITY, lsum = 0.f;
  f32x4 acc[8];
#pragma unroll
  for (int i = 0; i < 8; ++i) acc[i] = f32x4{0.f, 0.f, 0.f, 0.f};
  float sA[8], sB[8];
#pragma nounroll
  for (int it = 0; it < 4; ++it) {
    const bool nv = it < 3;
    const size_t o = (size_t)(nv ? 2 * it + 2 : 0) * 32768;
    qk_tile(A.k, qf, sA, kb + o, nv, koff);
    pv_tile(A.v, sA, m, lsum, acc, lane, vb + o, nv, voff);
    qk_tile(B.k, qf, sB, kb + o + 32768, nv, koff);
    pv_tile(B.v, sB, m, lsum, acc, lane, vb + o + 32768, nv, voff);
  }
  lsum = sum_rows(lsum);
  STAMP(5);
  float* om = (float*)(lds + L_OM);
  float* sm = (float*)(lds + L_SM);
  float* sl = (float*)(lds + L_SL);
  *(f32x4*)(om + w * 512 + lane * 8) = f32x4{acc[0][0], acc[1][0], acc[2][0], acc[3][0]};
  *(f32x4*)(om + w * 512 + lane * 8 + 4) = f32x4{acc[4][0], acc[5][0], acc[6][0], acc[7][0]};
  if (lane < 16 && (lane & 3) == 0) { sm[w * 4 + (lane >> 2)] = m; sl[w * 4 + (lane >> 2)] = lsum; }
  const int r8 = lane >> 3, c8 = lane & 7;

  if (w < 4) {
    v4u t16[16];
    const __amdgpu_buffer_rsrc_t rwo = __builtin_amdgcn_make_buffer_rsrc((void*)(p.wo + (size_t)(g * 512 + 64 * s + 16 * w) * DM), 0, (ABL_RED && w != b) ? 0 : 16384, RSRC_W3);
#pragma unroll
    for (int r = 0; r < 16; ++r) t16[r] = __builtin_amdgcn_raw_buffer_load_b128(rwo, voff + (r & 3) * 1024, (r >> 2) * 4096, AUX_W);
    v4u w2v[8];
    const int f0 = 256 * g + 64 * w;
    const __amdgpu_buffer_rsrc_t rw2 = __builtin_amdgcn_make_buffer_rsrc((void*)(p.w2 + (size_t)(g < 7 ? f0 : 0) * DM + 64 * s), 0, (g < 7 && !(ABL_RED && w != b)) ? 63 * 1024 + 128 : 0, RSRC_W3);
#pragma unroll
    for (int i = 0; i < 8; ++i) w2v[i] = __builtin_amdgcn_raw_buffer_load_b128(rw2, r8 * 1024 + c8 * 16, i * 8192, AUX_W);
    const u64* hsrc = ws + G_HX + b * 2048 + f0 + lane;
    u64 hg = gload(hsrc);
    SCHED();
#pragma unroll
    for (int r = 0; r < 16; ++r) *(v4u*)(lds + L_WO + (16 * w + r) * WO_LD + voff) = t16[r];
    if (g < 7) {
      if (!__all(gok(hg, tag))) {
        unsigned spin = 0;
        do { __builtin_amdgcn_s_sleep(PSLEEP); hg = gload(hsrc); } while (!__all(gok(hg, tag)) && ++spin < SPIN_CAP);
      }
      const float hl = bflo((unsigned)hg);
      float ma[8];
#pragma unroll
      for (int e = 0; e < 8; ++e) ma[e] = 0.f;
#pragma unroll
      for (int i = 0; i < 8; ++i) {
        const float hv = __shfl(hl, 8 * i + r8);
#pragma unroll
        for (int e = 0; e < 4; ++e) { ma[2 * e] = fmaf(hv, bflo(w2v[i][e]), ma[2 * e]); ma[2 * e + 1] = fmaf(hv, bfhi(w2v[i][e]), ma[2 * e + 1]); }
      }
      const float r = treduce8(ma, lane);
      gstore(ws + G_MX + ((size_t)(b * 64 + 8 * s + c8) * 32 + 4 * g + w) * 8 + r8, enc24(r), tag);
    }
    if (w == 1) {
      const unsigned b2p = *(const GAS unsigned*)(p.b2 + cb * 8 + 2 * (lane & 3));
      float m0, m1;
      gather8<2>(ws + G_MX + (size_t)(b * 64 + cb) * 256, 28, tag, lane, m0, m1);
      m0 = rbf(rbf(m0) + bflo(b2p)); m1 = rbf(rbf(m1) + bfhi(b2p));
      const float g0 = rbf(m0 / (1.0f + __expf(-m0))), g1 = rbf(m1 / (1.0f + __expf(-m1)));
      if (lane < 4) { ((float*)(lds + L_GATE))[2 * lane] = g0; ((float*)(lds + L_GATE))[2 * lane + 1] = g1; }
    }
  }
  STAMP(6);
#pragma nounroll
  for (int pass = (DRY && w == 2) ? 0 : 1; pass < 2; ++pass) {
  int real_v = pass;
  asm volatile("" : "+v"(real_v));
  const int real = __builtin_amdgcn_readfirstlane(real_v);
  u64* wsx = real ? ws : ws + G_SHADOW;
  u16* outx = real ? p.out : (u16*)(ws + G_SHADOW + 64);
  if (real) WG_BARRIER();
  {
    const int h = tid >> 7;
    float M = sm[h];
#pragma unroll
    for (int ww = 1; ww < 8; ++ww) M = fmaxf(M, sm[ww * 4 + h]);
    float O = 0.f, L = 0.f;
#pragma unroll
    for (int ww = 0; ww < 8; ++ww) {
      const float f = __builtin_amdgcn_exp2f(sm[ww * 4 + h] - M);
      O = fmaf(f, om[ww * 512 + tid], O); L = fmaf(f, sl[ww * 4 + h], L);
    }
    gstore(wsx + G_PX + (size_t)(pair * 8 + s) * 512 + tid, enc24(O), tag);
    const int d = tid & 127;
    if (d < 4) {
      const unsigned u = (d & 2) ? asu(L) : asu(M);
      gstore(wsx + G_PS + (size_t)((pair * 4 + h) * 8 + s) * 4 + d, (d & 1) ? (u & 0xffffu) : (u >> 16), tag);
    }
  }
  STAMP(7);
  {
    const bool pw = (w == PW) || !real;
    const __amdgpu_buffer_rsrc_t rp = __builtin_amdgcn_make_buffer_rsrc((void*)(wsx + G_PX + (size_t)pair * 4096 + 64 * s), 0, pw ? 8 * 4096 : 0, RSRC_W3);
    const __amdgpu_buffer_rsrc_t rs = __builtin_amdgcn_make_buffer_rsrc((void*)(wsx + G_PS + (size_t)(pair * 4 + (s >> 1)) * 32), 0, pw ? 256 : 0, RSRC_W3);
    const int poff = (lane >> 5) * 4096 + (lane & 31) * 16;
    v4u gp[4]; v2u gs;
#define PLOAD() do { _Pragma("unroll") for (int k = 0; k < 4; ++k) gp[k] = __builtin_amdgcn_raw_buffer_load_b128(rp, poff, k * 8192, AUX_SC1); \
      gs = __builtin_amdgcn_raw_buffer_load_b64(rs, (lane & 31) * 8, 0, AUX_SC1); } while (0)
    PLOAD();
    SCHED();
    float wf[8][8];
#pragma unroll
    for (int i = 0; i < 8; ++i) {
      const v4u t = *(const v4u*)(lds + L_WO + (8 * r8 + i) * WO_LD + w * 128 + c8 * 16);
#pragma unroll
      for (int e = 0; e < 4; ++e) { wf[i][2 * e] = bflo(t[e]); wf[i][2 * e + 1] = bfhi(t[e]); }
    }
#pragma unroll
    for (int i = 0; i < 8; ++i)
#pragma unroll
      for (int e = 0; e < 8; ++e) asm volatile("" : "+v"(wf[i][e]));
    SCHED();
    float* ov = (float*)(lds + L_OV + (real ? 0 : 256));
    if (pw) {
#define POK() __all(gok2(gp[0], tag) & gok2(gp[1], tag) & gok2(gp[2], tag) & gok2(gp[3], tag) & (gs.y == tag.hi) & ((gs.x & 0xff000000u) == tag.lo8))
      if (real && !POK()) {
        unsigned spin = 0;
        do {
          __builtin_amdgcn_s_sleep(PSLEEP);
          asm volatile("" ::: "memory");
          PLOAD();
        } while (!POK() && ++spin < SPIN_CAP);
      }
      STAMP(8);
      const unsigned sv = gs.x & 0xffffu;
      const float Mk = asf((asu(dpp<0x00>(asf(sv))) << 16) | asu(dpp<0x55>(asf(sv))));
      const float Lk = asf((asu(dpp<0xAA>(asf(sv))) << 16) | asu(dpp<0xFF>(asf(sv))));
      float M = fmaxf(Mk, dpp<ROR4>(Mk)); M = fmaxf(M, dpp<ROR8>(M));
      { auto t = __builtin_amdgcn_permlane16_swap(asu(M), asu(M), false, false); M = fmaxf(asf(t[0]), asf(t[1])); }
      const float fk = __builtin_amdgcn_exp2f(Mk - M);
      float L = fk * Lk; L += dpp<ROR4>(L); L += dpp<ROR8>(L); L = tr16(L, L);
      float o0 = 0.f, o1 = 0.f;
#pragma unroll
      for (int k = 0; k < 4; ++k) {
        const float fe = asf(__builtin_amdgcn_readlane(asu(fk), 8 * k)), fo = asf(__builtin_amdgcn_readlane(asu(fk), 8 * k + 4));
        const float f = (lane >> 5) ? fo : fe;
        o0 = fmaf(f, asf(gp[k].x << 8), o0); o1 = fmaf(f, asf(gp[k].z << 8), o1);
      }
      const float ot = tr32(o0, o1);
      ov[2 * (lane & 31) + (lane >> 5)] = rbf(ot / L);
      lds_set(lb + L_FO, real ? ltag : 0u);
    } else {
      lds_wait1(lb + L_FO, ltag);
    }
    asm volatile("s_waitcnt lgkmcnt(0)" ::: "memory");
    const f32x4 oa = *(const f32x4*)(ov + 8 * r8), ob4 = *(const f32x4*)(ov + 8 * r8 + 4);
    float sa[8];
#pragma unroll
    for (int e = 0; e < 8; ++e) {
      float t = oa[0] * wf[0][e];
      t = fmaf(oa[1], wf[1][e], t); t = fmaf(oa[2], wf[2][e], t); t = fmaf(oa[3], wf[3][e], t);
      t = fmaf(ob4[0], wf[4][e], t); t = fmaf(ob4[1], wf[5][e], t); t = fmaf(ob4[2], wf[6][e], t); t = fmaf(ob4[3], wf[7][e], t);
      sa[e] = t;
    }
    const float sp = treduce8(sa, lane);
    gstore(wsx + G_SX + ((size_t)(b * 64 + 8 * w + c8) * 64 + cb) * 8 + r8, enc24(sp), tag);
  }
  STAMP(9);
  if (w == 0 || !real) {
    const unsigned xp = *(const GAS unsigned*)(p.x + b * DM + cb * 8 + 2 * (lane & 3));
    const float2 gt = *(const float2*)(lds + L_GATE + 8 * (lane & 3));
    float s0, s1;
    gather8<4>(wsx + G_SX + (size_t)(b * 64 + cb) * 512, real ? 64 : 0, tag, lane, s0, s1);
    STAMP(10);
    const float o0 = bflo(xp) + rbf(rbf(s0) * gt.x), o1 = bfhi(xp) + rbf(rbf(s1) * gt.y);
    if (lane < 4) *(GAS unsigned*)(outx + b * DM + cb * 8 + 2 * lane) = cvtpk(o0, o1);
    if (bid == 0 && lane == 0) __hip_atomic_store((GAS u64*)(wsx + G_EPOCH), ((u64)tag.hi << 32) | tag.lo8, RLX_AGENT);
  }
  }
  STAMP(11);
  if (lane == 0) {
#pragma unroll
    for (int k = 0; k < NSTAMP; ++k) ws[STAMP_OFF / 8 + (bid * 8 + w) * NSTAMP + k] = stamp[k];
  }
}

__global__ void k_report(const u64* st, const void* k, const void* v, const void* ws) {
  if (threadIdx.x != 0) return;
  u64 t0 = ~0ull;
  for (int i = 0; i < 2048; ++i) if (st[i * NSTAMP] < t0) t0 = st[i * NSTAMP];
  printf("k %p v %p ws %p\n", k, v, ws);
  for (int kk = 0; kk < NSTAMP; ++kk) {
    u64 mn = ~0ull, mx = 0, sum = 0; int n = 0;
    for (int i = 0; i < 2048; ++i) { u64 t = st[i * NSTAMP + kk]; if (!t) continue; t -= t0; if (t < mn) mn = t; if (t > mx) mx = t; sum += t; ++n; }
    if (n) printf("s%d min %d avg %d max %d (x10ns) n %d\n", kk, (int)mn, (int)(sum / n), (int)mx, n);
  }
}
}

extern "C" void kernel_launch(void* const* d_in, const int* in_sizes, int n_in,
                              void* d_out, int out_size, void* d_ws, size_t ws_size,
                              hipStream_t stream) {
  Params p;
  p.x = (const u16*)d_in[0]; p.k = (const u16*)d_in[1]; p.v = (const u16*)d_in[2];
  p.wq = (const u16*)d_in[3]; p.wo = (const u16*)d_in[4];
  p.w1 = (const u16*)d_in[6]; p.b1 = (const u16*)d_in[7]; p.w2 = (const u16*)d_in[8]; p.b2 = (const u16*)d_in[9];
  p.out = (u16*)d_out; p.ws = (u64*)d_ws;
  hipLaunchKernelGGL(fused_decode, dim3(NWG), dim3(NTHR), 0, stream, p);
  hipLaunchKernelGGL(k_report, dim3(1), dim3(64), 0, stream, (const u64*)((char*)d_ws + STAMP_OFF), d_in[1], d_in[2], d_ws);
}
```

```cpp
#include <hip/hip_runtime.h>
#include <hip/hip_bf16.h>
#include <stdint.h>

namespace {

typedef unsigned v4u __attribute__((ext_vector_type(4)));
typedef unsigned v2u __attribute__((ext_vector_type(2)));
typedef float f32x4 __attribute__((ext_vector_type(4)));
typedef short bf16x8 __attribute__((ext_vector_type(8)));
typedef unsigned long long u64;
typedef unsigned short u16;
#define GAS __attribute__((address_space(1)))
#define RLX_AGENT __ATOMIC_RELAXED, __HIP_MEMORY_SCOPE_AGENT

constexpr int DM = 512, SK = 16384, FF = 1792;
constexpr int NWG = 256, NTHR = 512;
constexpr int W = 32;
constexpr int RSRC_W3 = 0x00020000;
constexpr int AUX_NT = 2, AUX_SC1 = 16;
#define AUX_W 0
#define AUX_K 2
#define AUX_V 2
#define TOPWIN 0
#define PW 1
#define AUX_W2 0
#define ABL_RED 0
#define DRY 0
#define AUX_G 16
#define PSLEEP 2
constexpr unsigned SPIN_CAP = 1u << 16;

constexpr size_t G_EPOCH = 0;
constexpr size_t G_QX = 512;
constexpr size_t G_PS = G_QX + 4 * 4096;
constexpr size_t G_PX = G_PS + 32 * 8 * 16;
constexpr size_t G_SX = G_PX + 32 * 8 * 512;
constexpr size_t G_HX = G_SX + 4 * 64 * 64 * 8;
constexpr size_t G_MX = G_HX + 4 * 2048;
constexpr size_t G_END = G_MX + 4 * 64 * 32 * 8;
constexpr size_t G_SHADOW = 524288;
static_assert(G_END <= G_SHADOW, "ws");

constexpr int WO_LD = 1040;
constexpr int L_OM = 0;
constexpr int L_XS = 16384, L_YS = L_XS + 8192;
constexpr int L_RED = 32768, L_REDH = L_RED + 1024, L_QS = L_REDH + 1024, L_SM = L_QS + 1024, L_SL = L_SM + 128,
              L_GATE = L_SL + 128, L_FA = L_GATE + 64, L_FB = L_FA + 16, L_FC = L_FB + 16, L_FD = L_FC + 16,
              L_FQ = L_FD + 16, L_TAG = L_FQ + 16, L_FO = L_TAG + 16, L_OV = L_FA + 128, L_WO = L_OV + 8 * 256, L_END = L_WO + 64 * WO_LD;
constexpr int LDS_BYTES = 108 * 1024;
static_assert(L_END <= LDS_BYTES && L_WO % 16 == 0 && L_OV % 16 == 0 && L_FA % 16 == 0, "lds");

struct Params {
  const u16* x; const u16* k; const u16* v; const u16* wq; const u16* wo;
  const u16* w1; const u16* b1; const u16* w2; const u16* b2;
  u16* out; u64* ws;
};

__device__ __forceinline__ float asf(unsigned u) { return __builtin_bit_cast(float, u); }
__device__ __forceinline__ unsigned asu(float f) { return __builtin_bit_cast(unsigned, f); }
__device__ __forceinline__ float bflo(unsigned u) { return asf(u << 16); }
__device__ __forceinline__ float bfhi(unsigned u) { return asf(u & 0xffff0000u); }
__device__ __forceinline__ unsigned f2bf(float f) { return (unsigned)__builtin_bit_cast(u16, (__bf16)f); }
__device__ __forceinline__ float rbf(float f) { return (float)(__bf16)f; }
__device__ __forceinline__ unsigned cvtpk(float lo, float hi) {
  typedef __bf16 bf2 __attribute__((ext_vector_type(2)));
  bf2 r; r.x = (__bf16)lo; r.y = (__bf16)hi;
  return __builtin_bit_cast(unsigned, r);
}
template <int CTRL> __device__ __forceinline__ float dpp(float x) {
  return __builtin_bit_cast(float, __builtin_amdgcn_mov_dpp(__builtin_bit_cast(int, x), CTRL, 0xf, 0xf, true));
}
constexpr int XOR1 = 0xB1, XOR2 = 0x4E, HMIR = 0x141, MIR = 0x140, ROR8 = 0x128, ROR4 = 0x124;
__device__ __forceinline__ float tr32(float a, float b) {
  auto r = __builtin_amdgcn_permlane32_swap(asu(a), asu(b), false, false);
  return asf(r[0]) + asf(r[1]);
}
__device__ __forceinline__ float tr16(float a, float b) {
  auto r = __builtin_amdgcn_permlane16_swap(asu(a), asu(b), false, false);
  return asf(r[0]) + asf(r[1]);
}
__device__ __forceinline__ float max_rows(float x) {
  auto s = __builtin_amdgcn_permlane16_swap(asu(x), asu(x), false, false);
  x = fmaxf(asf(s[0]), asf(s[1]));
  auto t = __builtin_amdgcn_permlane32_swap(asu(x), asu(x), false, false);
  return fmaxf(asf(t[0]), asf(t[1]));
}
__device__ __forceinline__ float sum_rows(float x) { x = tr16(x, x); return tr32(x, x); }
__device__ __forceinline__ float wave_sum(float x) {
  x += dpp<XOR1>(x); x += dpp<XOR2>(x); x += dpp<HMIR>(x); x += dpp<MIR>(x);
  return sum_rows(x);
}
__device__ __forceinline__ float treduce8(const float (&v)[8], int lane) {
  const float u0 = tr32(v[0], v[4]), u1 = tr32(v[1], v[5]), u2 = tr32(v[2], v[6]), u3 = tr32(v[3], v[7]);
  const float w0 = tr16(u0, u2), w1 = tr16(u1, u3);
  const float t0 = w0 + dpp<ROR8>(w0), t1 = w1 + dpp<ROR8>(w1);
  return (lane & 8) ? t1 : t0;
}
__device__ __forceinline__ void ln_row(const float (&v)[8], float (&o)[8]) {
  float s = 0.f;
#pragma unroll
  for (int i = 0; i < 8; ++i) s += v[i];
  s = wave_sum(s);
  const float mean = s * (1.0f / DM);
  float ss = 0.f;
#pragma unroll
  for (int i = 0; i < 8; ++i) { const float d = v[i] - mean; ss = fmaf(d, d, ss); }
  ss = wave_sum(ss);
  const float var_b = rbf(ss * (1.0f / DM)), m_b = rbf(mean);
  const float den = rbf(sqrtf(rbf(var_b + 1e-5f)));
#pragma unroll
  for (int i = 0; i < 8; ++i) o[i] = rbf(rbf(v[i] - m_b) / den);
}
#define WG_BARRIER() asm volatile("s_waitcnt lgkmcnt(0)\n\ts_barrier" ::: "memory")
#define SCHED() __builtin_amdgcn_sched_barrier(0)
#define STAMP(k) do {} while (0)

struct Tag { unsigned lo8, hi; };
__device__ __forceinline__ void gstore(u64* p, unsigned payload24, Tag t) {
  __hip_atomic_store((GAS u64*)p, ((u64)t.hi << 32) | (u64)(payload24 | t.lo8), RLX_AGENT);
}
__device__ __forceinline__ u64 gload(const u64* p) { return __hip_atomic_load((const GAS u64*)p, RLX_AGENT); }
__device__ __forceinline__ bool gok(u64 g, Tag t) {
  return (unsigned)(g >> 32) == t.hi && ((unsigned)g & 0xff000000u) == t.lo8;
}
__device__ __forceinline__ unsigned gbad2(v4u g, Tag t) {
  return (((g.x ^ t.lo8) | (g.z ^ t.lo8)) & 0xff000000u) | (g.y ^ t.hi) | (g.w ^ t.hi);
}
__device__ __forceinline__ bool gok2(v4u g, Tag t) { return gbad2(g, t) == 0u; }
__device__ __forceinline__ unsigned enc24(float f) { unsigned u = asu(f); u += 0x7fu + ((u >> 8) & 1u); return u >> 8; }
__device__ __forceinline__ float dec24(u64 g) { return asf((unsigned)g << 8); }

__device__ __forceinline__ f32x4 mfma(v4u a, v4u b, f32x4 c) {
  return __builtin_amdgcn_mfma_f32_16x16x32_bf16(__builtin_bit_cast(bf16x8, a), __builtin_bit_cast(bf16x8, b), c, 0, 0, 0);
}

__device__ __forceinline__ float gemv16(const unsigned (&wv)[16], const float* vec, int rg, int lane) {
  float acc[8];
#pragma unroll
  for (int bb = 0; bb < 4; ++bb) {
    float a0 = 0.f, a1 = 0.f;
#pragma unroll
    for (int q4 = 0; q4 < 4; ++q4) {
      const f32x4 x0 = *(const f32x4*)(vec + bb * DM + 16 * rg + 4 * q4);
#pragma unroll
      for (int r = 0; r < 4; ++r) { a0 = fmaf(x0[r], bflo(wv[4 * q4 + r]), a0); a1 = fmaf(x0[r], bfhi(wv[4 * q4 + r]), a1); }
    }
    acc[bb * 2] = a0; acc[bb * 2 + 1] = a1;
  }
  return treduce8(acc, lane);
}
__device__ __forceinline__ void lds_set(unsigned addr, unsigned v) {
  asm volatile("s_waitcnt lgkmcnt(0)\n\tds_write_b32 %0, %1" :: "v"(addr), "v"(v) : "memory");
}
__device__ __forceinline__ void lds_wait4(unsigned addr, unsigned v) {
#pragma nounroll
  for (unsigned spin = 0; spin < (1u << 22); ++spin) {
    v4u f;
    asm volatile("ds_read_b128 %0, %1\n\ts_waitcnt lgkmcnt(0)" : "=v"(f) : "v"(addr) : "memory");
    if (__builtin_amdgcn_readfirstlane((f.x == v) & (f.y == v) & (f.z == v) & (f.w == v))) break;
    __builtin_amdgcn_s_sleep(1);
  }
}
__device__ __forceinline__ void lds_wait1(unsigned addr, unsigned v) {
#pragma nounroll
  for (unsigned spin = 0; spin < (1u << 22); ++spin) {
    unsigned f;
    asm volatile("ds_read_b32 %0, %1\n\ts_waitcnt lgkmcnt(0)" : "=v"(f) : "v"(addr) : "memory");
    if (__builtin_amdgcn_readfirstlane(f) == v) break;
    __builtin_amdgcn_s_sleep(1);
  }
}

struct Tile { v4u k[8]; v4u v[8]; };
__device__ __forceinline__ void ld8(v4u (&d)[8], const u16* base, bool valid, int off) {
  const __amdgpu_buffer_rsrc_t r = __builtin_amdgcn_make_buffer_rsrc((void*)base, 0, valid ? 8192 : 0, RSRC_W3);
#pragma unroll
  for (int i = 0; i < 8; ++i) d[i] = __builtin_amdgcn_raw_buffer_load_b128(r, off + (i & 3) * 1024, (i >> 2) * 4096, AUX_NT);
  SCHED();
}
__device__ __forceinline__ void qk_tile(v4u (&k)[8], v4u qf, float (&sc)[8], const u16* kn, bool nvalid, int koff) {
  constexpr float C2 = 0.08838834764831845f * 1.4426950408889634f;
  f32x4 d[8];
#pragma unroll
  for (int i = 0; i < 8; ++i) d[i] = mfma(k[i], qf, f32x4{0.f, 0.f, 0.f, 0.f});
  SCHED();
  ld8(k, kn, nvalid, koff);
#pragma unroll
  for (int i = 0; i < 8; ++i) sc[i] = ((dpp<0x00>(d[i][0]) + dpp<0x55>(d[i][1])) + (dpp<0xAA>(d[i][2]) + dpp<0xFF>(d[i][3]))) * C2;
}
__device__ __forceinline__ void pv_tile(v4u (&v)[8], const float (&sc)[8], float& m, float& l, f32x4 (&acc)[8], int lane,
                                        const u16* vn, bool nvalid, int voff) {
  float tm = fmaxf(fmaxf(fmaxf(sc[0], sc[1]), fmaxf(sc[2], sc[3])), fmaxf(fmaxf(sc[4], sc[5]), fmaxf(sc[6], sc[7])));
  tm = max_rows(tm);
  if (__any(tm > m)) {
    const float mn = fmaxf(m, tm), al = __builtin_amdgcn_exp2f(m - mn);
    m = mn; l *= al;
    const float ah = __shfl(al, (lane >> 4) * 4);
#pragma unroll
    for (int j = 0; j < 8; ++j) acc[j] *= ah;
  }
  v4u pk;
#pragma unroll
  for (int k = 0; k < 4; ++k) {
    const float p0 = __builtin_amdgcn_exp2f(sc[2 * k] - m), p1 = __builtin_amdgcn_exp2f(sc[2 * k + 1] - m);
    l += p0 + p1; pk[k] = cvtpk(p0, p1);
  }
#pragma unroll
  for (int jj = 0; jj < 4; ++jj) {
    v4u lo, hi;
#pragma unroll
    for (int k2 = 0; k2 < 4; ++k2) {
      lo[k2] = __builtin_amdgcn_perm(v[2 * k2 + 1][jj], v[2 * k2][jj], 0x05040100u);
      hi[k2] = __builtin_amdgcn_perm(v[2 * k2 + 1][jj], v[2 * k2][jj], 0x07060302u);
    }
    acc[2 * jj] = mfma(pk, lo, acc[2 * jj]); acc[2 * jj + 1] = mfma(pk, hi, acc[2 * jj + 1]);
  }
  SCHED();
  ld8(v, vn, nvalid, voff);
}
template <int NK> __device__ __forceinline__ void poll_pairs(__amdgpu_buffer_rsrc_t r, int voff, int kstride, unsigned skip, Tag tag, v4u (&gv)[NK]) {
  int off[NK];
#pragma unroll
  for (int k = 0; k < NK; ++k) { off[k] = ((skip >> k) & 1u) ? 0x40000000 : voff; gv[k] = v4u{0u, 0u, 0u, 0u}; }
#pragma nounroll
  for (unsigned spin = 0; spin < SPIN_CAP; ++spin) {
    asm volatile("" ::: "memory");
    v4u t[NK];
#pragma unroll
    for (int k = 0; k < NK; ++k) t[k] = __builtin_amdgcn_raw_buffer_load_b128(r, off[k], k * kstride, AUX_SC1);
    bool pend = false;
#pragma unroll
    for (int k = 0; k < NK; ++k) {
      const bool got = gbad2(t[k], tag) == 0u;
      const bool live = off[k] == voff;
      if (got & live) { gv[k].x = t[k].x; gv[k].z = t[k].z; off[k] = 0x40000000; }
      pend |= live & !got;
    }
    if (!__any(pend)) break;
    __builtin_amdgcn_s_sleep(PSLEEP);
  }
}
template <int NK> __device__ __forceinline__ void gather8(const u64* src, int nparts, Tag tag, int lane, float& s0, float& s1) {
  const __amdgpu_buffer_rsrc_t r = __builtin_amdgcn_make_buffer_rsrc((void*)src, 0, NK * 1024, RSRC_W3);
  v4u gv[NK];
  unsigned skip = 0u;
#pragma unroll
  for (int k = 0; k < NK; ++k) skip |= (16 * k + (lane >> 2) >= nparts) ? (1u << k) : 0u;
  poll_pairs<NK>(r, lane * 16, 1024, skip, tag, gv);
  s0 = 0.f; s1 = 0.f;
#pragma unroll
  for (int k = 0; k < NK; ++k) { s0 += asf(gv[k].x << 8); s1 += asf(gv[k].z << 8); }
  s0 += dpp<ROR4>(s0); s1 += dpp<ROR4>(s1);
  s0 += dpp<ROR8>(s0); s1 += dpp<ROR8>(s1);
  s0 = sum_rows(s0); s1 = sum_rows(s1);
}

__global__ void __launch_bounds__(NTHR, 2) fused_decode(Params p) {
  __shared__ __attribute__((aligned(16))) unsigned char lds[LDS_BYTES];
  const int tid = threadIdx.x, lane = tid & 63, w = __builtin_amdgcn_readfirstlane(tid >> 6);
  const int bid = blockIdx.x, g = bid & 7, j = bid >> 3, b = j >> 3, s = j & 7, pair = b * 8 + g, cb = g * 8 + s;
  u64* ws = p.ws;
  const unsigned lb = (unsigned)(size_t)(__attribute__((address_space(3))) unsigned char*)lds;
  STAMP(0);

  if (tid < 32) ((unsigned*)(lds + L_FA))[tid] = 0u;
  WG_BARRIER();
  const size_t kvoff = ((size_t)pair * SK + s * 2048 + w * 32) * 128;
  const u16* kb = p.k + kvoff;
  const u16* vb = p.v + kvoff;
  const int koff = ((lane & 15) >> 2) * 256 + (lane & 3) * 16 + (lane >> 4) * 64, voff = lane * 16;
  constexpr unsigned ltag = 1u;
  Tile A, B;
#define ISSUE_WINDOW(on) do { ld8(A.k, kb, on, koff); ld8(A.v, vb, on, voff); ld8(B.k, kb + 32768, on, koff); ld8(B.v, vb + 32768, on, voff); } while (0)
  if (w < 4) {
    const u64 e_raw = gload(ws + G_EPOCH);
    const v4u xr = AUX_W ? __builtin_nontemporal_load((const GAS v4u*)(p.x + w * DM + 8 * lane)) : *(const GAS v4u*)(p.x + w * DM + 8 * lane);
    const int cp = tid & 7, rg = tid >> 3;
    const bool hwg = j >= 18;
    const int f0 = 224 * g + 16 * (hwg ? j - 18 : 0);
    unsigned wqv[16], w1v[16];
    {
      const __amdgpu_buffer_rsrc_t rq = __builtin_amdgcn_make_buffer_rsrc((void*)(p.wq + g * 512 + 16 * j), 0, 512 * 8192 - 8192 + 32, RSRC_W3);
#pragma unroll
      for (int r = 0; r < 16; ++r) wqv[r] = __builtin_amdgcn_raw_buffer_load_b32(rq, rg * (16 * 8192) + cp * 4, r * 8192, AUX_W);
      const __amdgpu_buffer_rsrc_t r1 = __builtin_amdgcn_make_buffer_rsrc((void*)(p.w1 + f0), 0, hwg ? 511 * FF * 2 + 32 : 0, RSRC_W3);
#pragma unroll
      for (int r = 0; r < 16; ++r) w1v[r] = __builtin_amdgcn_raw_buffer_load_b32(r1, rg * (16 * FF * 2) + cp * 4, r * FF * 2, AUX_W);
    }
    const int ob = lane >> 4, ocol = 2 * (lane & 7) + ((lane >> 3) & 1);
    const unsigned b1v = p.b1[f0 + ocol];
    SCHED();
    float* xs = (float*)(lds + L_XS);
    float* ys = (float*)(lds + L_YS);
    float* red = (float*)(lds + L_RED);
    float* redh = (float*)(lds + L_REDH);
    float xv[8], xn[8];
#pragma unroll
    for (int i = 0; i < 4; ++i) { xv[2 * i] = bflo(xr[i]); xv[2 * i + 1] = bfhi(xr[i]); }
    ln_row(xv, xn);
    *(f32x4*)(xs + w * DM + 8 * lane) = f32x4{xn[0], xn[1], xn[2], xn[3]};
    *(f32x4*)(xs + w * DM + 8 * lane + 4) = f32x4{xn[4], xn[5], xn[6], xn[7]};
    const u64 e_ = ((u64)__builtin_amdgcn_readfirstlane((unsigned)(e_raw >> 32)) << 32) | (u64)__builtin_amdgcn_readfirstlane((unsigned)e_raw);
    const u64 tag40 = ((e_ >> 24) * 0x5DEECE66DULL + 0xBULL) & ((1ULL << 40) - 1);
    Tag tag; tag.lo8 = (unsigned)(tag40 & 0xff) << 24; tag.hi = (unsigned)(tag40 >> 8);
    lds_set(lb + L_FA + 4 * w, ltag);
    STAMP(1);
    lds_wait4(lb + L_FA, ltag);
    red[w * 64 + lane] = gemv16(wqv, xs, rg, lane);
    lds_set(lb + L_FB + 4 * w, ltag);
    const __amdgpu_buffer_rsrc_t rqx = __builtin_amdgcn_make_buffer_rsrc((void*)(ws + G_QX + b * 4096 + g * 512), 0, 4096, RSRC_W3);
    v4u gq[4];
    if (w == 0) {
      lds_wait4(lb + L_FB, ltag);
      const float q = (red[lane] + red[64 + lane]) + (red[128 + lane] + red[192 + lane]);
      gstore(ws + G_QX + ob * 4096 + g * 512 + 16 * j + ocol, f2bf(q), tag);
      STAMP(2);
    }
    if (hwg) {
      float y[8];
      ln_row(xn, y);
      *(f32x4*)(ys + w * DM + 8 * lane) = f32x4{y[0], y[1], y[2], y[3]};
      *(f32x4*)(ys + w * DM + 8 * lane + 4) = f32x4{y[4], y[5], y[6], y[7]};
      lds_set(lb + L_FC + 4 * w, ltag);
      lds_wait4(lb + L_FC, ltag);
      redh[w * 64 + lane] = gemv16(w1v, ys, rg, lane);
      lds_set(lb + L_FD + 4 * w, ltag);
      if (w == 1) {
        lds_wait4(lb + L_FD, ltag);
        const float hp = (redh[lane] + redh[64 + lane]) + (redh[128 + lane] + redh[192 + lane]);
        const float pre = rbf(rbf(hp) + bflo(b1v));
        const float hv = 0.5f * pre * (1.0f + tanhf(0.7978845608028654f * (pre + 0.044715f * pre * pre * pre)));
        gstore(ws + G_HX + ob * 2048 + f0 + ocol, f2bf(hv), tag);
      }
    }
    if (w == 0) {
      poll_pairs<4>(rqx, lane * 16, 1024, 0u, tag, gq);
#pragma unroll
      for (int k = 0; k < 4; ++k) ((unsigned*)(lds + L_QS))[64 * k + lane] = (gq[k].x & 0xffffu) | (gq[k].z << 16);
      *(v2u*)(lds + L_TAG) = v2u{tag.lo8, tag.hi};
      lds_set(lb + L_FQ, ltag);
      STAMP(3);
    }
  }
  SCHED();
  ISSUE_WINDOW(true);
  lds_wait1(lb + L_FQ, ltag);
  Tag tag;
  { const v2u t = *(const v2u*)(lds + L_TAG); tag.lo8 = __builtin_amdgcn_readfirstlane(t.x); tag.hi = __builtin_amdgcn_readfirstlane(t.y); }
  STAMP(4);
  const v4u qf = *(const v4u*)(lds + L_QS + ((lane & 15) >> 2) * 256 + ((lane & 3) + 4 * (lane >> 4)) * 16);

  float m = -INFINITY, lsum = 0.f;
  f32x4 acc[8];
#pragma unroll
  for (int i = 0; i < 8; ++i) acc[i] = f32x4{0.f, 0.f, 0.f, 0.f};
  float sA[8], sB[8];
#pragma nounroll
  for (int it = 0; it < 4; ++it) {
    const bool nv = it < 3;
    const size_t o = (size_t)(nv ? 2 * it + 2 : 0) * 32768;
    qk_tile(A.k, qf, sA, kb + o, nv, koff);
    pv_tile(A.v, sA, m, lsum, acc, lane, vb + o, nv, voff);
    qk_tile(B.k, qf, sB, kb + o + 32768, nv, koff);
    pv_tile(B.v, sB, m, lsum, acc, lane, vb + o + 32768, nv, voff);
  }
  lsum = sum_rows(lsum);
  STAMP(5);
  float* om = (float*)(lds + L_OM);
  float* sm = (float*)(lds + L_SM);
  float* sl = (float*)(lds + L_SL);
  *(f32x4*)(om + w * 512 + lane * 8) = f32x4{acc[0][0], acc[1][0], acc[2][0], acc[3][0]};
  *(f32x4*)(om + w * 512 + lane * 8 + 4) = f32x4{acc[4][0], acc[5][0], acc[6][0], acc[7][0]};
  if (lane < 16 && (lane & 3) == 0) { sm[(lane >> 2) * 8 + w] = m; sl[(lane >> 2) * 8 + w] = lsum; }
  const int r8 = lane >> 3, c8 = lane & 7;

  v4u bw[2][8];
  if (w < 4) {
    v4u t16[16];
    const __amdgpu_buffer_rsrc_t rwo = __builtin_amdgcn_make_buffer_rsrc((void*)(p.wo + (size_t)(g * 512 + 64 * s) * DM + 128 * w), 0, (ABL_RED & 1) ? 0 : 63 * 1024 + 256, RSRC_W3);
#pragma unroll
    for (int r = 0; r < 16; ++r) t16[r] = __builtin_amdgcn_raw_buffer_load_b128(rwo, (lane >> 4) * 1024 + (lane & 15) * 16, r * 4096, AUX_W2);
    v4u w2v[8];
    const int f0 = 256 * g + 64 * w;
    const __amdgpu_buffer_rsrc_t rw2 = __builtin_amdgcn_make_buffer_rsrc((void*)(p.w2 + (size_t)(g < 7 ? f0 : 0) * DM + 64 * s), 0, (g < 7 && !(ABL_RED & 2)) ? 63 * 1024 + 128 : 0, RSRC_W3);
#pragma unroll
    for (int i = 0; i < 8; ++i) w2v[i] = __builtin_amdgcn_raw_buffer_load_b128(rw2, r8 * 1024 + c8 * 16, i * 8192, AUX_W2);
    const u64* hsrc = ws + G_HX + b * 2048 + f0 + lane;
    u64 hg = gload(hsrc);
    SCHED();
#pragma unroll
    for (int ks = 0; ks < 2; ++ks)
#pragma unroll
      for (int jj = 0; jj < 4; ++jj)
#pragma unroll
        for (int k2 = 0; k2 < 4; ++k2) {
          bw[ks][2 * jj][k2] = __builtin_amdgcn_perm(t16[8 * ks + 2 * k2 + 1][jj], t16[8 * ks + 2 * k2][jj], 0x05040100u);
          bw[ks][2 * jj + 1][k2] = __builtin_amdgcn_perm(t16[8 * ks + 2 * k2 + 1][jj], t16[8 * ks + 2 * k2][jj], 0x07060302u);
        }
    if (g < 7 && !(ABL_RED & 4)) {
      if (!__all(gok(hg, tag))) {
        unsigned spin = 0;
        do { __builtin_amdgcn_s_sleep(PSLEEP); hg = gload(hsrc); } while (!__all(gok(hg, tag)) && ++spin < SPIN_CAP);
      }
      const float hl = bflo((unsigned)hg);
      float ma[8];
#pragma unroll
      for (int e = 0; e < 8; ++e) ma[e] = 0.f;
#pragma unroll
      for (int i = 0; i < 8; ++i) {
        const float hv = __shfl(hl, 8 * i + r8);
#pragma unroll
        for (int e = 0; e < 4; ++e) { ma[2 * e] = fmaf(hv, bflo(w2v[i][e]), ma[2 * e]); ma[2 * e + 1] = fmaf(hv, bfhi(w2v[i][e]), ma[2 * e + 1]); }
      }
      const float r = treduce8(ma, lane);
      gstore(ws + G_MX + ((size_t)(b * 64 + 8 * s + c8) * 32 + 4 * g + w) * 8 + r8, enc24(r), tag);
    }
  }
  STAMP(6);
  const unsigned xp = *(const GAS unsigned*)(p.x + b * DM + cb * 8 + 2 * (lane & 3));
  WG_BARRIER();
  {
    const int h = tid >> 7;
    const f32x4 m0 = *(const f32x4*)(sm + h * 8), m1 = *(const f32x4*)(sm + h * 8 + 4);
    float ov[8];
#pragma unroll
    for (int ww = 0; ww < 8; ++ww) ov[ww] = om[ww * 512 + tid];
    const f32x4 l0 = *(const f32x4*)(sl + h * 8), l1 = *(const f32x4*)(sl + h * 8 + 4);
    SCHED();
    const float M = fmaxf(fmaxf(fmaxf(m0[0], m0[1]), fmaxf(m0[2], m0[3])), fmaxf(fmaxf(m1[0], m1[1]), fmaxf(m1[2], m1[3])));
    float O = 0.f, L = 0.f;
#pragma unroll
    for (int ww = 0; ww < 4; ++ww) { const float f = __builtin_amdgcn_exp2f(m0[ww] - M); O = fmaf(f, ov[ww], O); L = fmaf(f, l0[ww], L); }
#pragma unroll
    for (int ww = 0; ww < 4; ++ww) { const float f = __builtin_amdgcn_exp2f(m1[ww] - M); O = fmaf(f, ov[4 + ww], O); L = fmaf(f, l1[ww], L); }
    gstore(ws + G_PX + (size_t)(pair * 8 + s) * 512 + tid, enc24(O), tag);
    const int d = tid & 127;
    if (d < 4) {
      const unsigned u = (d & 2) ? asu(L) : asu(M);
      gstore(ws + G_PS + (size_t)((pair * 4 + h) * 8 + s) * 4 + d, (d & 1) ? (u & 0xffffu) : (u >> 16), tag);
    }
  }
  STAMP(7);
  if (w == 4) {
    const __amdgpu_buffer_rsrc_t rp = __builtin_amdgcn_make_buffer_rsrc((void*)(ws + G_PX + (size_t)pair * 4096 + 64 * s), 0, 8 * 4096, RSRC_W3);
    const __amdgpu_buffer_rsrc_t rs = __builtin_amdgcn_make_buffer_rsrc((void*)(ws + G_PS + (size_t)(pair * 4 + (s >> 1)) * 32), 0, 256, RSRC_W3);
    const int poff = (lane >> 5) * 4096 + (lane & 31) * 16;
    v4u gp[4]; v2u gs;
#pragma nounroll
    for (unsigned spin = 0; spin < SPIN_CAP; ++spin) {
      asm volatile("" ::: "memory");
      gs = __builtin_amdgcn_raw_buffer_load_b64(rs, (lane & 31) * 8, 0, AUX_SC1);
#pragma unroll
      for (int k = 0; k < 4; ++k) gp[k] = __builtin_amdgcn_raw_buffer_load_b128(rp, poff, k * 8192, AUX_SC1);
      const unsigned bad = gbad2(gp[0], tag) | gbad2(gp[1], tag) | gbad2(gp[2], tag) | gbad2(gp[3], tag) | (gs.y ^ tag.hi) | ((gs.x ^ tag.lo8) & 0xff000000u);
      if (__all(bad == 0u)) break;
      __builtin_amdgcn_s_sleep(PSLEEP);
    }
    STAMP(8);
    const unsigned sv = gs.x & 0xffffu;
    const float Mk = asf((asu(dpp<0x00>(asf(sv))) << 16) | asu(dpp<0x55>(asf(sv))));
    const float Lk = asf((asu(dpp<0xAA>(asf(sv))) << 16) | asu(dpp<0xFF>(asf(sv))));
    float M = fmaxf(Mk, dpp<ROR4>(Mk)); M = fmaxf(M, dpp<ROR8>(M));
    { auto t = __builtin_amdgcn_permlane16_swap(asu(M), asu(M), false, false); M = fmaxf(asf(t[0]), asf(t[1])); }
    const float fk = __builtin_amdgcn_exp2f(Mk - M);
    float L = fk * Lk; L += dpp<ROR4>(L); L += dpp<ROR8>(L); L = tr16(L, L);
    float o0 = 0.f, o1 = 0.f;
#pragma unroll
    for (int k = 0; k < 4; ++k) {
      const float fe = asf(__builtin_amdgcn_readlane(asu(fk), 8 * k)), fo = asf(__builtin_amdgcn_readlane(asu(fk), 8 * k + 4));
      const float f = (lane >> 5) ? fo : fe;
      o0 = fmaf(f, asf(gp[k].x << 8), o0); o1 = fmaf(f, asf(gp[k].z << 8), o1);
    }
    const float ot = tr32(o0, o1);
    const int e = 2 * (lane & 31) + (lane >> 5);
    ((u16*)(lds + L_OV))[(e & 32) + (e & 3) * 8 + ((e >> 2) & 7)] = (u16)f2bf(ot / L);
    lds_set(lb + L_FO, ltag);
  } else if (w < 4) {
    const __amdgpu_buffer_rsrc_t rsx = __builtin_amdgcn_make_buffer_rsrc((void*)(ws + G_SX + ((size_t)(b * 64 + 16 * w) * 64 + cb) * 8), 0, 16 * 4096, RSRC_W3);
    lds_wait1(lb + L_FO, ltag);
    const v4u a0 = *(const v4u*)(lds + L_OV + (lane >> 4) * 16), a1 = *(const v4u*)(lds + L_OV + 64 + (lane >> 4) * 16);
    f32x4 d[8];
#pragma unroll
    for (int jx = 0; jx < 8; ++jx) d[jx] = mfma(a0, bw[0][jx], f32x4{0.f, 0.f, 0.f, 0.f});
#pragma unroll
    for (int jx = 0; jx < 8; ++jx) d[jx] = mfma(a1, bw[1][jx], d[jx]);
    const int g4 = lane >> 4;
    const float v0 = (g4 & 2) ? ((g4 & 1) ? d[6][0] : d[4][0]) : ((g4 & 1) ? d[2][0] : d[0][0]);
    const float v1 = (g4 & 2) ? ((g4 & 1) ? d[7][0] : d[5][0]) : ((g4 & 1) ? d[3][0] : d[1][0]);
    __builtin_amdgcn_raw_buffer_store_b128(v4u{enc24(v0) | tag.lo8, tag.hi, enc24(v1) | tag.lo8, tag.hi}, rsx, (lane & 15) * 4096 + g4 * 16, 0, AUX_SC1);
    STAMP(9);
  } else if (w == 5) {
    const unsigned b2p = *(const GAS unsigned*)(p.b2 + cb * 8 + 2 * (lane & 3));
    float m0, m1;
    gather8<2>(ws + G_MX + (size_t)(b * 64 + cb) * 256, 28, tag, lane, m0, m1);
    m0 = rbf(rbf(m0) + bflo(b2p)); m1 = rbf(rbf(m1) + bfhi(b2p));
    float2 gt;
    gt.x = rbf(m0 / (1.0f + __expf(-m0))); gt.y = rbf(m1 / (1.0f + __expf(-m1)));
    float s0, s1;
    gather8<4>(ws + G_SX + (size_t)(b * 64 + cb) * 512, 64, tag, lane, s0, s1);
    STAMP(10);
    const float o0 = bflo(xp) + rbf(rbf(s0) * gt.x), o1 = bfhi(xp) + rbf(rbf(s1) * gt.y);
    if (lane < 4) *(GAS unsigned*)(p.out + b * DM + cb * 8 + 2 * lane) = cvtpk(o0, o1);
    if (bid == 0 && lane == 0) __hip_atomic_store((GAS u64*)(ws + G_EPOCH), ((u64)tag.hi << 32) | tag.lo8, RLX_AGENT);
  }
}

}

extern "C" void kernel_launch(void* const* d_in, const int* in_sizes, int n_in,
                              void* d_out, int out_size, void* d_ws, size_t ws_size,
                              hipStream_t stream) {
  Params p;
  p.x = (const u16*)d_in[0]; p.k = (const u16*)d_in[1]; p.v = (const u16*)d_in[2];
  p.wq = (const u16*)d_in[3]; p.wo = (const u16*)d_in[4];
  p.w1 = (const u16*)d_in[6]; p.b1 = (const u16*)d_in[7]; p.w2 = (const u16*)d_in[8]; p.b2 = (const u16*)d_in[9];
  p.out = (u16*)d_out; p.ws = (u64*)d_ws;
  hipLaunchKernelGGL(fused_decode, dim3(NWG), dim3(NTHR), 0, stream, p);
}
```

```cpp
#include <hip/hip_runtime.h>
#include <hip/hip_bf16.h>
#include <stdint.h>

namespace {

typedef unsigned v4u __attribute__((ext_vector_type(4)));
typedef unsigned v2u __attribute__((ext_vector_type(2)));
typedef float f32x4 __attribute__((ext_vector_type(4)));
typedef short bf16x8 __attribute__((ext_vector_type(8)));
typedef unsigned long long u64;
typedef unsigned short u16;
#define GAS __attribute__((address_space(1)))
#define RLX_AGENT __ATOMIC_RELAXED, __HIP_MEMORY_SCOPE_AGENT

constexpr int DM = 512, SK = 16384, FF = 1792;
constexpr int NWG = 256, NTHR = 512;
constexpr int W = 32;
constexpr int RSRC_W3 = 0x00020000;
constexpr int AUX_NT = 2, AUX_SC1 = 16;
#define AUX_W 0
#define AUX_K 2
#define AUX_V 2
#define TOPWIN 1
#define PW 1
#define AUX_W2 0
#define LATEK 0
#define ABL_T 0
#define XMAP 2
#define SKT 0
#define ABL_RED 0
#define DRY 0
#define AUX_G 16
#define PSLEEP 2
constexpr unsigned SPIN_CAP = 1u << 16;

constexpr size_t G_EPOCH = 0;
constexpr size_t G_QX = 512;
constexpr size_t G_PS = G_QX + 4 * 4096;
constexpr size_t G_PX = G_PS + 32 * 8 * 16;
constexpr size_t G_SX = G_PX + 32 * 8 * 512;
constexpr size_t G_HX = G_SX + 4 * 64 * 64 * 8;
constexpr size_t G_MX = G_HX + 4 * 2048;
constexpr size_t G_END = G_MX + 4 * 64 * 32 * 8;
constexpr size_t G_SHADOW = 524288;
static_assert(G_END <= G_SHADOW, "ws");

constexpr int WO_LD = 1040;
constexpr int L_OM = 0;
constexpr int L_XS = 16384, L_YS = L_XS + 8192;
constexpr int L_RED = 32768, L_REDH = L_RED + 1024, L_QS = L_REDH + 1024, L_SM = L_QS + 1024, L_SL = L_SM + 128,
              L_GATE = L_SL + 128, L_FA = L_GATE + 64, L_FB = L_FA + 16, L_FC = L_FB + 16, L_FD = L_FC + 16,
              L_FQ = L_FD + 16, L_TAG = L_FQ + 16, L_FO = L_TAG + 16, L_OV = L_FA + 128, L_WO = L_OV + 8 * 256, L_END = L_WO + 64 * WO_LD;
constexpr int LDS_BYTES = 108 * 1024;
static_assert(L_END <= LDS_BYTES && L_WO % 16 == 0 && L_OV % 16 == 0 && L_FA % 16 == 0, "lds");

struct Params {
  const u16* x; const u16* k; const u16* v; const u16* wq; const u16* wo;
  const u16* w1; const u16* b1; const u16* w2; const u16* b2;
  u16* out; u64* ws;
};

__device__ __forceinline__ float asf(unsigned u) { return __builtin_bit_cast(float, u); }
__device__ __forceinline__ unsigned asu(float f) { return __builtin_bit_cast(unsigned, f); }
__device__ __forceinline__ float bflo(unsigned u) { return asf(u << 16); }
__device__ __forceinline__ float bfhi(unsigned u) { return asf(u & 0xffff0000u); }
__device__ __forceinline__ unsigned f2bf(float f) { return (unsigned)__builtin_bit_cast(u16, (__bf16)f); }
__device__ __forceinline__ float rbf(float f) { return (float)(__bf16)f; }
__device__ __forceinline__ unsigned cvtpk(float lo, float hi) {
  typedef __bf16 bf2 __attribute__((ext_vector_type(2)));
  bf2 r; r.x = (__bf16)lo; r.y = (__bf16)hi;
  return __builtin_bit_cast(unsigned, r);
}
template <int CTRL> __device__ __forceinline__ float dpp(float x) {
  return __builtin_bit_cast(float, __builtin_amdgcn_mov_dpp(__builtin_bit_cast(int, x), CTRL, 0xf, 0xf, true));
}
constexpr int XOR1 = 0xB1, XOR2 = 0x4E, HMIR = 0x141, MIR = 0x140, ROR8 = 0x128, ROR4 = 0x124;
__device__ __forceinline__ float tr32(float a, float b) {
  auto r = __builtin_amdgcn_permlane32_swap(asu(a), asu(b), false, false);
  return asf(r[0]) + asf(r[1]);
}
__device__ __forceinline__ float tr16(float a, float b) {
  auto r = __builtin_amdgcn_permlane16_swap(asu(a), asu(b), false, false);
  return asf(r[0]) + asf(r[1]);
}
__device__ __forceinline__ float max_rows(float x) {
  auto s = __builtin_amdgcn_permlane16_swap(asu(x), asu(x), false, false);
  x = fmaxf(asf(s[0]), asf(s[1]));
  auto t = __builtin_amdgcn_permlane32_swap(asu(x), asu(x), false, false);
  return fmaxf(asf(t[0]), asf(t[1]));
}
__device__ __forceinline__ float sum_rows(float x) { x = tr16(x, x); return tr32(x, x); }
__device__ __forceinline__ float wave_sum(float x) {
  x += dpp<XOR1>(x); x += dpp<XOR2>(x); x += dpp<HMIR>(x); x += dpp<MIR>(x);
  return sum_rows(x);
}
__device__ __forceinline__ float treduce8(const float (&v)[8], int lane) {
  const float u0 = tr32(v[0], v[4]), u1 = tr32(v[1], v[5]), u2 = tr32(v[2], v[6]), u3 = tr32(v[3], v[7]);
  const float w0 = tr16(u0, u2), w1 = tr16(u1, u3);
  const float t0 = w0 + dpp<ROR8>(w0), t1 = w1 + dpp<ROR8>(w1);
  return (lane & 8) ? t1 : t0;
}
__device__ __forceinline__ void ln_row(const float (&v)[8], float (&o)[8]) {
  float s = 0.f;
#pragma unroll
  for (int i = 0; i < 8; ++i) s += v[i];
  s = wave_sum(s);
  const float mean = s * (1.0f / DM);
  float ss = 0.f;
#pragma unroll
  for (int i = 0; i < 8; ++i) { const float d = v[i] - mean; ss = fmaf(d, d, ss); }
  ss = wave_sum(ss);
  const float var_b = rbf(ss * (1.0f / DM)), m_b = rbf(mean);
  const float den = rbf(sqrtf(rbf(var_b + 1e-5f)));
#pragma unroll
  for (int i = 0; i < 8; ++i) o[i] = rbf(rbf(v[i] - m_b) / den);
}
#define WG_BARRIER() asm volatile("s_waitcnt lgkmcnt(0)\n\ts_barrier" ::: "memory")
#define SCHED() __builtin_amdgcn_sched_barrier(0)
#define STAMP(k) do {} while (0)

struct Tag { unsigned lo8, hi; };
__device__ __forceinline__ void gstore(u64* p, unsigned payload24, Tag t) {
  __hip_atomic_store((GAS u64*)p, ((u64)t.hi << 32) | (u64)(payload24 | t.lo8), RLX_AGENT);
}
__device__ __forceinline__ u64 gload(const u64* p) { return __hip_atomic_load((const GAS u64*)p, RLX_AGENT); }
__device__ __forceinline__ bool gok(u64 g, Tag t) {
  return (unsigned)(g >> 32) == t.hi && ((unsigned)g & 0xff000000u) == t.lo8;
}
__device__ __forceinline__ unsigned gbad2(v4u g, Tag t) {
  return (((g.x ^ t.lo8) | (g.z ^ t.lo8)) & 0xff000000u) | (g.y ^ t.hi) | (g.w ^ t.hi);
}
__device__ __forceinline__ bool gok2(v4u g, Tag t) { return gbad2(g, t) == 0u; }
__device__ __forceinline__ unsigned enc24(float f) { unsigned u = asu(f); u += 0x7fu + ((u >> 8) & 1u); return u >> 8; }
__device__ __forceinline__ float dec24(u64 g) { return asf((unsigned)g << 8); }

__device__ __forceinline__ f32x4 mfma(v4u a, v4u b, f32x4 c) {
  return __builtin_amdgcn_mfma_f32_16x16x32_bf16(__builtin_bit_cast(bf16x8, a), __builtin_bit_cast(bf16x8, b), c, 0, 0, 0);
}

__device__ __forceinline__ float gemv16(const unsigned (&wv)[16], const float* vec, int rg, int lane) {
  float acc[8];
#pragma unroll
  for (int bb = 0; bb < 4; ++bb) {
    float a0 = 0.f, a1 = 0.f;
#pragma unroll
    for (int q4 = 0; q4 < 4; ++q4) {
      const f32x4 x0 = *(const f32x4*)(vec + bb * DM + 16 * rg + 4 * q4);
#pragma unroll
      for (int r = 0; r < 4; ++r) { a0 = fmaf(x0[r], bflo(wv[4 * q4 + r]), a0); a1 = fmaf(x0[r], bfhi(wv[4 * q4 + r]), a1); }
    }
    acc[bb * 2] = a0; acc[bb * 2 + 1] = a1;
  }
  return treduce8(acc, lane);
}
__device__ __forceinline__ void lds_set(unsigned addr, unsigned v) {
  asm volatile("s_waitcnt lgkmcnt(0)\n\tds_write_b32 %0, %1" :: "v"(addr), "v"(v) : "memory");
}
__device__ __forceinline__ void lds_wait4(unsigned addr, unsigned v) {
#pragma nounroll
  for (unsigned spin = 0; spin < (1u << 22); ++spin) {
    v4u f;
    asm volatile("ds_read_b128 %0, %1\n\ts_waitcnt lgkmcnt(0)" : "=v"(f) : "v"(addr) : "memory");
    if (__builtin_amdgcn_readfirstlane((f.x == v) & (f.y == v) & (f.z == v) & (f.w == v))) break;
    __builtin_amdgcn_s_sleep(1);
  }
}
__device__ __forceinline__ void lds_wait1(unsigned addr, unsigned v) {
#pragma nounroll
  for (unsigned spin = 0; spin < (1u << 22); ++spin) {
    unsigned f;
    asm volatile("ds_read_b32 %0, %1\n\ts_waitcnt lgkmcnt(0)" : "=v"(f) : "v"(addr) : "memory");
    if (__builtin_amdgcn_readfirstlane(f) == v) break;
    __builtin_amdgcn_s_sleep(1);
  }
}

struct Tile { v4u k[8]; v4u v[8]; };
__device__ __forceinline__ void ld8(v4u (&d)[8], const u16* base, bool valid, int off) {
  const __amdgpu_buffer_rsrc_t r = __builtin_amdgcn_make_buffer_rsrc((void*)base, 0, valid ? 8192 : 0, RSRC_W3);
#pragma unroll
  for (int i = 0; i < 8; ++i) d[i] = __builtin_amdgcn_raw_buffer_load_b128(r, off + (i & 3) * 1024, (i >> 2) * 4096, AUX_NT);
  SCHED();
}
__device__ __forceinline__ void qk_tile(v4u (&k)[8], v4u qf, float (&sc)[8], const u16* kn, bool nvalid, int koff, float bias) {
  constexpr float C2 = 0.08838834764831845f * 1.4426950408889634f;
  f32x4 d[8];
#pragma unroll
  for (int i = 0; i < 8; ++i) d[i] = mfma(k[i], qf, f32x4{0.f, 0.f, 0.f, 0.f});
  SCHED();
  ld8(k, kn, nvalid, koff);
#pragma unroll
  for (int i = 0; i < 8; ++i) sc[i] = fmaf((dpp<0x00>(d[i][0]) + dpp<0x55>(d[i][1])) + (dpp<0xAA>(d[i][2]) + dpp<0xFF>(d[i][3])), C2, bias);
}
__device__ __forceinline__ void pv_tile(v4u (&v)[8], const float (&sc)[8], float& m, float& l, f32x4 (&acc)[8], int lane,
                                        const u16* vn, bool nvalid, int voff, v4u (&k)[8], const u16* kn, int koff) {
  float tm = fmaxf(fmaxf(fmaxf(sc[0], sc[1]), fmaxf(sc[2], sc[3])), fmaxf(fmaxf(sc[4], sc[5]), fmaxf(sc[6], sc[7])));
  tm = max_rows(tm);
  if (__any(tm > m)) {
    const float mn = fmaxf(m, tm), al = __builtin_amdgcn_exp2f(m - mn);
    m = mn; l *= al;
    const float ah = __shfl(al, (lane >> 4) * 4);
#pragma unroll
    for (int j = 0; j < 8; ++j) acc[j] *= ah;
  }
  v4u pk;
#pragma unroll
  for (int k = 0; k < 4; ++k) {
    const float p0 = __builtin_amdgcn_exp2f(sc[2 * k] - m), p1 = __builtin_amdgcn_exp2f(sc[2 * k + 1] - m);
    l += p0 + p1; pk[k] = cvtpk(p0, p1);
  }
#pragma unroll
  for (int jj = 0; jj < 4; ++jj) {
    v4u lo, hi;
#pragma unroll
    for (int k2 = 0; k2 < 4; ++k2) {
      lo[k2] = __builtin_amdgcn_perm(v[2 * k2 + 1][jj], v[2 * k2][jj], 0x05040100u);
      hi[k2] = __builtin_amdgcn_perm(v[2 * k2 + 1][jj], v[2 * k2][jj], 0x07060302u);
    }
    acc[2 * jj] = mfma(pk, lo, acc[2 * jj]); acc[2 * jj + 1] = mfma(pk, hi, acc[2 * jj + 1]);
  }
  SCHED();
  ld8(v, vn, nvalid, voff);
}
template <int NK> __device__ __forceinline__ void poll_pairs(__amdgpu_buffer_rsrc_t r, int voff, int kstride, unsigned skip, Tag tag, v4u (&gv)[NK]) {
  int off[NK];
#pragma unroll
  for (int k = 0; k < NK; ++k) { off[k] = ((skip >> k) & 1u) ? 0x40000000 : voff; gv[k] = v4u{0u, 0u, 0u, 0u}; }
#pragma nounroll
  for (unsigned spin = 0; spin < SPIN_CAP; ++spin) {
    asm volatile("" ::: "memory");
    v4u t[NK];
#pragma unroll
    for (int k = 0; k < NK; ++k) t[k] = __builtin_amdgcn_raw_buffer_load_b128(r, off[k], k * kstride, AUX_SC1);
    bool pend = false;
#pragma unroll
    for (int k = 0; k < NK; ++k) {
      const bool got = gbad2(t[k], tag) == 0u;
      const bool live = off[k] == voff;
      if (got & live) { gv[k].x = t[k].x; gv[k].z = t[k].z; off[k] = 0x40000000; }
      pend |= live & !got;
    }
    if (!__any(pend)) break;
    __builtin_amdgcn_s_sleep(PSLEEP);
  }
}
template <int NK> __device__ __forceinline__ void gather8(const u64* src, int nparts, Tag tag, int lane, float& s0, float& s1) {
  const __amdgpu_buffer_rsrc_t r = __builtin_amdgcn_make_buffer_rsrc((void*)src, 0, NK * 1024, RSRC_W3);
  v4u gv[NK];
  unsigned skip = 0u;
#pragma unroll
  for (int k = 0; k < NK; ++k) skip |= (16 * k + (lane >> 2) >= nparts) ? (1u << k) : 0u;
  poll_pairs<NK>(r, lane * 16, 1024, skip, tag, gv);
  s0 = 0.f; s1 = 0.f;
#pragma unroll
  for (int k = 0; k < NK; ++k) { s0 += asf(gv[k].x << 8); s1 += asf(gv[k].z << 8); }
  s0 += dpp<ROR4>(s0); s1 += dpp<ROR4>(s1);
  s0 += dpp<ROR8>(s0); s1 += dpp<ROR8>(s1);
  s0 = sum_rows(s0); s1 = sum_rows(s1);
}

__global__ void __launch_bounds__(NTHR, 2) fused_decode(Params p) {
  __shared__ __attribute__((aligned(16))) unsigned char lds[LDS_BYTES];
  const int tid = threadIdx.x, lane = tid & 63, w = __builtin_amdgcn_readfirstlane(tid >> 6);
  const int bid = blockIdx.x, g = bid & 7, j = bid >> 3, b = j >> 3, s = j & 7, pair = b * 8 + g, cb = g * 8 + s;
  const int kbt = g >> 1, kg = j >> 2, ks = (j & 3) * 2 + (g & 1);
  const int tile0 = (j & 3) * 128 + ((g & 1) ? 64 - SKT : 0), ntl = (g & 1) ? 64 + SKT : 64 - SKT;
  const int kpair = kbt * 8 + kg, nmine = (ntl - w + 7) >> 3;
  u64* ws = p.ws;
  const unsigned lb = (unsigned)(size_t)(__attribute__((address_space(3))) unsigned char*)lds;
  STAMP(0);

  if (tid < 32) ((unsigned*)(lds + L_FA))[tid] = 0u;
  const size_t kvoff = ((size_t)kpair * SK + (tile0 + w) * 32) * 128;
  const u16* kb = p.k + kvoff;
  const u16* vb = p.v + kvoff;
  const int koff = ((lane & 15) >> 2) * 256 + (lane & 3) * 16 + (lane >> 4) * 64, voff = lane * 16;
  constexpr unsigned ltag = 1u;
  Tile A, B;
#define ISSUE_WINDOW(on) do { ld8(A.k, kb, on, koff); ld8(A.v, vb, on, voff); ld8(B.k, kb + 32768, on, koff); ld8(B.v, vb + 32768, on, voff); } while (0)
  const int cp = tid & 7, rg = tid >> 3;
  const bool hwg = j >= 18;
  const int f0 = 224 * g + 16 * (hwg ? j - 18 : 0);
  const int ob = lane >> 4, ocol = 2 * (lane & 7) + ((lane >> 3) & 1);
  u64 e_raw; v4u xr; unsigned wqv[16], w1v[16];
  int wA = w, wB = w, wC = w;
  asm volatile("" : "+s"(wA)); asm volatile("" : "+s"(wB)); asm volatile("" : "+s"(wC));
  if (wA < 4) {
    e_raw = gload(ws + G_EPOCH);
    xr = *(const GAS v4u*)(p.x + w * DM + 8 * lane);
    const __amdgpu_buffer_rsrc_t rq = __builtin_amdgcn_make_buffer_rsrc((void*)(p.wq + g * 512 + 16 * j), 0, 512 * 8192 - 8192 + 32, RSRC_W3);
#pragma unroll
    for (int r = 0; r < 16; ++r) wqv[r] = __builtin_amdgcn_raw_buffer_load_b32(rq, rg * (16 * 8192) + cp * 4, r * 8192, AUX_W);
    const __amdgpu_buffer_rsrc_t r1 = __builtin_amdgcn_make_buffer_rsrc((void*)(p.w1 + f0), 0, hwg ? 511 * FF * 2 + 32 : 0, RSRC_W3);
#pragma unroll
    for (int r = 0; r < 16; ++r) w1v[r] = __builtin_amdgcn_raw_buffer_load_b32(r1, rg * (16 * FF * 2) + cp * 4, r * FF * 2, AUX_W);
    SCHED();
  }
  WG_BARRIER();
  if (wB >= 4) ISSUE_WINDOW(true);
  if (wC < 4) {
    const unsigned b1v = p.b1[f0 + ocol];
    SCHED();
    float* xs = (float*)(lds + L_XS);
    float* ys = (float*)(lds + L_YS);
    float* red = (float*)(lds + L_RED);
    float* redh = (float*)(lds + L_REDH);
    float xv[8], xn[8];
#pragma unroll
    for (int i = 0; i < 4; ++i) { xv[2 * i] = bflo(xr[i]); xv[2 * i + 1] = bfhi(xr[i]); }
    ln_row(xv, xn);
    *(f32x4*)(xs + w * DM + 8 * lane) = f32x4{xn[0], xn[1], xn[2], xn[3]};
    *(f32x4*)(xs + w * DM + 8 * lane + 4) = f32x4{xn[4], xn[5], xn[6], xn[7]};
    const u64 e_ = ((u64)__builtin_amdgcn_readfirstlane((unsigned)(e_raw >> 32)) << 32) | (u64)__builtin_amdgcn_readfirstlane((unsigned)e_raw);
    const u64 tag40 = ((e_ >> 24) * 0x5DEECE66DULL + 0xBULL) & ((1ULL << 40) - 1);
    Tag tag; tag.lo8 = (unsigned)(tag40 & 0xff) << 24; tag.hi = (unsigned)(tag40 >> 8);
    lds_set(lb + L_FA + 4 * w, ltag);
    STAMP(1);
    lds_wait4(lb + L_FA, ltag);
    red[w * 64 + lane] = gemv16(wqv, xs, rg, lane);
    lds_set(lb + L_FB + 4 * w, ltag);
    const __amdgpu_buffer_rsrc_t rqx = __builtin_amdgcn_make_buffer_rsrc((void*)(ws + G_QX + kbt * 4096 + kg * 512), 0, 4096, RSRC_W3);
    v4u gq[4];
    if (w == 0) {
      lds_wait4(lb + L_FB, ltag);
      const float q = (red[lane] + red[64 + lane]) + (red[128 + lane] + red[192 + lane]);
      gstore(ws + G_QX + ob * 4096 + g * 512 + 16 * j + ocol, f2bf(q), tag);
      STAMP(2);
    }
    if (hwg) {
      float y[8];
      ln_row(xn, y);
      *(f32x4*)(ys + w * DM + 8 * lane) = f32x4{y[0], y[1], y[2], y[3]};
      *(f32x4*)(ys + w * DM + 8 * lane + 4) = f32x4{y[4], y[5], y[6], y[7]};
      lds_set(lb + L_FC + 4 * w, ltag);
      lds_wait4(lb + L_FC, ltag);
      redh[w * 64 + lane] = gemv16(w1v, ys, rg, lane);
      lds_set(lb + L_FD + 4 * w, ltag);
      if (w == 1) {
        lds_wait4(lb + L_FD, ltag);
        const float hp = (redh[lane] + redh[64 + lane]) + (redh[128 + lane] + redh[192 + lane]);
        const float pre = rbf(rbf(hp) + bflo(b1v));
        const float hv = 0.5f * pre * (1.0f + tanhf(0.7978845608028654f * (pre + 0.044715f * pre * pre * pre)));
        gstore(ws + G_HX + ob * 2048 + f0 + ocol, f2bf(hv), tag);
      }
    }
    if (w == 0) {
      poll_pairs<4>(rqx, lane * 16, 1024, 0u, tag, gq);
#pragma unroll
      for (int k = 0; k < 4; ++k) ((unsigned*)(lds + L_QS))[64 * k + lane] = (gq[k].x & 0xffffu) | (gq[k].z << 16);
      *(v2u*)(lds + L_TAG) = v2u{tag.lo8, tag.hi};
      lds_set(lb + L_FQ, ltag);
      STAMP(3);
    }
    SCHED();
    ISSUE_WINDOW(true);
  }
  lds_wait1(lb + L_FQ, ltag);
  Tag tag;
  { const v2u t = *(const v2u*)(lds + L_TAG); tag.lo8 = __builtin_amdgcn_readfirstlane(t.x); tag.hi = __builtin_amdgcn_readfirstlane(t.y); }
  STAMP(4);
  const v4u qf = *(const v4u*)(lds + L_QS + ((lane & 15) >> 2) * 256 + ((lane & 3) + 4 * (lane >> 4)) * 16);

  float m = -INFINITY, lsum = 0.f;
  f32x4 acc[8];
#pragma unroll
  for (int i = 0; i < 8; ++i) acc[i] = f32x4{0.f, 0.f, 0.f, 0.f};
  float sA[8], sB[8];
#pragma nounroll
  for (int t = 0; t < nmine; t += 2) {
    const bool na = t + 2 < nmine, nb = t + 3 < nmine;
    const size_t oa = (size_t)(na ? t + 2 : 0) * 32768, ob2 = (size_t)(nb ? t + 3 : 0) * 32768;
    qk_tile(A.k, qf, sA, kb + oa, na, koff, 0.f);
    pv_tile(A.v, sA, m, lsum, acc, lane, vb + oa, na, voff, A.k, kb + oa, koff);
    qk_tile(B.k, qf, sB, kb + ob2, nb, koff, t + 1 < nmine ? 0.f : -INFINITY);
    pv_tile(B.v, sB, m, lsum, acc, lane, vb + ob2, nb, voff, B.k, kb + ob2, koff);
  }
  lsum = sum_rows(lsum);
  STAMP(5);
  float* om = (float*)(lds + L_OM);
  float* sm = (float*)(lds + L_SM);
  float* sl = (float*)(lds + L_SL);
  *(f32x4*)(om + w * 512 + lane * 8) = f32x4{acc[0][0], acc[1][0], acc[2][0], acc[3][0]};
  *(f32x4*)(om + w * 512 + lane * 8 + 4) = f32x4{acc[4][0], acc[5][0], acc[6][0], acc[7][0]};
  if (lane < 16 && (lane & 3) == 0) { sm[(lane >> 2) * 8 + w] = m; sl[(lane >> 2) * 8 + w] = lsum; }
  const int r8 = lane >> 3, c8 = lane & 7;

  v4u bw[2][8];
  if (w < 4) {
    v4u t16[16];
    const __amdgpu_buffer_rsrc_t rwo = __builtin_amdgcn_make_buffer_rsrc((void*)(p.wo + (size_t)(g * 512 + 64 * s) * DM + 128 * w), 0, ((ABL_RED & 1) || ((ABL_RED & 8) && w != b)) ? 0 : 63 * 1024 + 256, RSRC_W3);
#pragma unroll
    for (int r = 0; r < 16; ++r) t16[r] = __builtin_amdgcn_raw_buffer_load_b128(rwo, (lane >> 4) * 1024 + (lane & 15) * 16, r * 4096, AUX_W2);
    v4u w2v[8];
    const int f0 = 256 * g + 64 * w;
    const __amdgpu_buffer_rsrc_t rw2 = __builtin_amdgcn_make_buffer_rsrc((void*)(p.w2 + (size_t)(g < 7 ? f0 : 0) * DM + 64 * s), 0, (g < 7 && !(ABL_RED & 2) && !((ABL_RED & 16) && w != b)) ? 63 * 1024 + 128 : 0, RSRC_W3);
#pragma unroll
    for (int i = 0; i < 8; ++i) w2v[i] = __builtin_amdgcn_raw_buffer_load_b128(rw2, r8 * 1024 + c8 * 16, i * 8192, AUX_W2);
    const u64* hsrc = ws + G_HX + b * 2048 + f0 + lane;
    u64 hg = gload(hsrc);
    SCHED();
#pragma unroll
    for (int ks = 0; ks < 2; ++ks)
#pragma unroll
      for (int jj = 0; jj < 4; ++jj)
#pragma unroll
        for (int k2 = 0; k2 < 4; ++k2) {
          bw[ks][2 * jj][k2] = __builtin_amdgcn_perm(t16[8 * ks + 2 * k2 + 1][jj], t16[8 * ks + 2 * k2][jj], 0x05040100u);
          bw[ks][2 * jj + 1][k2] = __builtin_amdgcn_perm(t16[8 * ks + 2 * k2 + 1][jj], t16[8 * ks + 2 * k2][jj], 0x07060302u);
        }
    if (g < 7 && !(ABL_RED & 4)) {
      if (!__all(gok(hg, tag))) {
        unsigned spin = 0;
        do { __builtin_amdgcn_s_sleep(PSLEEP); hg = gload(hsrc); } while (!__all(gok(hg, tag)) && ++spin < SPIN_CAP);
      }
      const float hl = bflo((unsigned)hg);
      float ma[8];
#pragma unroll
      for (int e = 0; e < 8; ++e) ma[e] = 0.f;
#pragma unroll
      for (int i = 0; i < 8; ++i) {
        const float hv = __shfl(hl, 8 * i + r8);
#pragma unroll
        for (int e = 0; e < 4; ++e) { ma[2 * e] = fmaf(hv, bflo(w2v[i][e]), ma[2 * e]); ma[2 * e + 1] = fmaf(hv, bfhi(w2v[i][e]), ma[2 * e + 1]); }
      }
      const float r = treduce8(ma, lane);
      gstore(ws + G_MX + ((size_t)(b * 64 + 8 * s + c8) * 32 + 4 * g + w) * 8 + r8, enc24(r), tag);
    }
  }
  STAMP(6);
  const unsigned xp = *(const GAS unsigned*)(p.x + b * DM + cb * 8 + 2 * (lane & 3));
  WG_BARRIER();
  {
    const int h = tid >> 7;
    const f32x4 m0 = *(const f32x4*)(sm + h * 8), m1 = *(const f32x4*)(sm + h * 8 + 4);
    float ov[8];
#pragma unroll
    for (int ww = 0; ww < 8; ++ww) ov[ww] = om[ww * 512 + tid];
    const f32x4 l0 = *(const f32x4*)(sl + h * 8), l1 = *(const f32x4*)(sl + h * 8 + 4);
    SCHED();
    const float M = fmaxf(fmaxf(fmaxf(m0[0], m0[1]), fmaxf(m0[2], m0[3])), fmaxf(fmaxf(m1[0], m1[1]), fmaxf(m1[2], m1[3])));
    float O = 0.f, L = 0.f;
#pragma unroll
    for (int ww = 0; ww < 4; ++ww) { const float f = __builtin_amdgcn_exp2f(m0[ww] - M); O = fmaf(f, ov[ww], O); L = fmaf(f, l0[ww], L); }
#pragma unroll
    for (int ww = 0; ww < 4; ++ww) { const float f = __builtin_amdgcn_exp2f(m1[ww] - M); O = fmaf(f, ov[4 + ww], O); L = fmaf(f, l1[ww], L); }
    gstore(ws + G_PX + (size_t)(kpair * 8 + ks) * 512 + tid, enc24(O), tag);
    const int d = tid & 127;
    if (d < 4) {
      const unsigned u = (d & 2) ? asu(L) : asu(M);
      gstore(ws + G_PS + (size_t)((kpair * 4 + h) * 8 + ks) * 4 + d, (d & 1) ? (u & 0xffffu) : (u >> 16), tag);
    }
  }
  STAMP(7);
  if (ABL_T & 2) {} else if (w == 4) {
    const __amdgpu_buffer_rsrc_t rp = __builtin_amdgcn_make_buffer_rsrc((void*)(ws + G_PX + (size_t)pair * 4096 + 64 * s), 0, 8 * 4096, RSRC_W3);
    const __amdgpu_buffer_rsrc_t rs = __builtin_amdgcn_make_buffer_rsrc((void*)(ws + G_PS + (size_t)(pair * 4 + (s >> 1)) * 32), 0, 256, RSRC_W3);
    const int poff = (lane >> 5) * 4096 + (lane & 31) * 16;
    v4u gp[4]; v2u gs;
#pragma nounroll
    for (unsigned spin = 0; spin < SPIN_CAP; ++spin) {
      asm volatile("" ::: "memory");
      gs = __builtin_amdgcn_raw_buffer_load_b64(rs, (lane & 31) * 8, 0, AUX_SC1);
#pragma unroll
      for (int k = 0; k < 4; ++k) gp[k] = __builtin_amdgcn_raw_buffer_load_b128(rp, poff, k * 8192, AUX_SC1);
      const unsigned bad = gbad2(gp[0], tag) | gbad2(gp[1], tag) | gbad2(gp[2], tag) | gbad2(gp[3], tag) | (gs.y ^ tag.hi) | ((gs.x ^ tag.lo8) & 0xff000000u);
      if (__all(bad == 0u)) break;
      __builtin_amdgcn_s_sleep(PSLEEP);
    }
    STAMP(8);
    const unsigned sv = gs.x & 0xffffu;
    const float Mk = asf((asu(dpp<0x00>(asf(sv))) << 16) | asu(dpp<0x55>(asf(sv))));
    const float Lk = asf((asu(dpp<0xAA>(asf(sv))) << 16) | asu(dpp<0xFF>(asf(sv))));
    float M = fmaxf(Mk, dpp<ROR4>(Mk)); M = fmaxf(M, dpp<ROR8>(M));
    { auto t = __builtin_amdgcn_permlane16_swap(asu(M), asu(M), false, false); M = fmaxf(asf(t[0]), asf(t[1])); }
    const float fk = __builtin_amdgcn_exp2f(Mk - M);
    float L = fk * Lk; L += dpp<ROR4>(L); L += dpp<ROR8>(L); L = tr16(L, L);
    float o0 = 0.f, o1 = 0.f;
#pragma unroll
    for (int k = 0; k < 4; ++k) {
      const float fe = asf(__builtin_amdgcn_readlane(asu(fk), 8 * k)), fo = asf(__builtin_amdgcn_readlane(asu(fk), 8 * k + 4));
      const float f = (lane >> 5) ? fo : fe;
      o0 = fmaf(f, asf(gp[k].x << 8), o0); o1 = fmaf(f, asf(gp[k].z << 8), o1);
    }
    const float ot = tr32(o0, o1);
    const int e = 2 * (lane & 31) + (lane >> 5);
    ((u16*)(lds + L_OV))[(e & 32) + (e & 3) * 8 + ((e >> 2) & 7)] = (u16)f2bf(ot / L);
    lds_set(lb + L_FO, ltag);
  } else if (w < 4) {
    const __amdgpu_buffer_rsrc_t rsx = __builtin_amdgcn_make_buffer_rsrc((void*)(ws + G_SX + ((size_t)(b * 64 + 16 * w) * 64 + cb) * 8), 0, 16 * 4096, RSRC_W3);
    lds_wait1(lb + L_FO, ltag);
    const v4u a0 = *(const v4u*)(lds + L_OV + (lane >> 4) * 16), a1 = *(const v4u*)(lds + L_OV + 64 + (lane >> 4) * 16);
    f32x4 d[8];
#pragma unroll
    for (int jx = 0; jx < 8; ++jx) d[jx] = mfma(a0, bw[0][jx], f32x4{0.f, 0.f, 0.f, 0.f});
#pragma unroll
    for (int jx = 0; jx < 8; ++jx) d[jx] = mfma(a1, bw[1][jx], d[jx]);
    const int g4 = lane >> 4;
    const float v0 = (g4 & 2) ? ((g4 & 1) ? d[6][0] : d[4][0]) : ((g4 & 1) ? d[2][0] : d[0][0]);
    const float v1 = (g4 & 2) ? ((g4 & 1) ? d[7][0] : d[5][0]) : ((g4 & 1) ? d[3][0] : d[1][0]);
    __builtin_amdgcn_raw_buffer_store_b128(v4u{enc24(v0) | tag.lo8, tag.hi, enc24(v1) | tag.lo8, tag.hi}, rsx, (lane & 15) * 4096 + g4 * 16, 0, AUX_SC1);
    STAMP(9);
  } else if (w == 5 && !(ABL_T & 1)) {
    const unsigned b2p = *(const GAS unsigned*)(p.b2 + cb * 8 + 2 * (lane & 3));
    float m0, m1;
    gather8<2>(ws + G_MX + (size_t)(b * 64 + cb) * 256, 28, tag, lane, m0, m1);
    m0 = rbf(rbf(m0) + bflo(b2p)); m1 = rbf(rbf(m1) + bfhi(b2p));
    float2 gt;
    gt.x = rbf(m0 / (1.0f + __expf(-m0))); gt.y = rbf(m1 / (1.0f + __expf(-m1)));
    float s0, s1;
    gather8<4>(ws + G_SX + (size_t)(b * 64 + cb) * 512, 64, tag, lane, s0, s1);
    STAMP(10);
    const float o0 = bflo(xp) + rbf(rbf(s0) * gt.x), o1 = bfhi(xp) + rbf(rbf(s1) * gt.y);
    if (lane < 4) *(GAS unsigned*)(p.out + b * DM + cb * 8 + 2 * lane) = cvtpk(o0, o1);
    if (bid == 0 && lane == 0) __hip_atomic_store((GAS u64*)(ws + G_EPOCH), ((u64)tag.hi << 32) | tag.lo8, RLX_AGENT);
  }
}

}

extern "C" void kernel_launch(void* const* d_in, const int* in_sizes, int n_in,
                              void* d_out, int out_size, void* d_ws, size_t ws_size,
                              hipStream_t stream) {
  Params p;
  p.x = (const u16*)d_in[0]; p.k = (const u16*)d_in[1]; p.v = (const u16*)d_in[2];
  p.wq = (const u16*)d_in[3]; p.wo = (const u16*)d_in[4];
  p.w1 = (const u16*)d_in[6]; p.b1 = (const u16*)d_in[7]; p.w2 = (const u16*)d_in[8]; p.b2 = (const u16*)d_in[9];
  p.out = (u16*)d_out; p.ws = (u64*)d_ws;
  hipLaunchKernelGGL(fused_decode, dim3(NWG), dim3(NTHR), 0, stream, p);
}
```

```cpp
#include <hip/hip_runtime.h>
#include <hip/hip_bf16.h>
#include <stdint.h>

namespace {

typedef unsigned v4u __attribute__((ext_vector_type(4)));
typedef unsigned v2u __attribute__((ext_vector_type(2)));
typedef float f32x4 __attribute__((ext_vector_type(4)));
typedef short bf16x8 __attribute__((ext_vector_type(8)));
typedef unsigned long long u64;
typedef unsigned short u16;
#define GAS __attribute__((address_space(1)))
#define RLX_AGENT __ATOMIC_RELAXED, __HIP_MEMORY_SCOPE_AGENT

constexpr int DM = 512, SK = 16384, FF = 1792;
constexpr int NWG = 256, NTHR = 512;
constexpr int W = 32;
constexpr int RSRC_W3 = 0x00020000;
constexpr int AUX_NT = 2, AUX_SC1 = 16;
#define AUX_W 0
#define AUX_K 2
#define AUX_V 2
#define TOPWIN 0
#define PW 1
#define AUX_W2 0
#define LATEK 0
#define ABL_T 0
#define XMAP 1
#define SKT 0
#define PRET 65536
#define ABL_RED 0
#define DRY 0
#define AUX_G 16
#define PSLEEP 2
constexpr unsigned SPIN_CAP = 1u << 16;

constexpr size_t G_EPOCH = 0;
constexpr size_t G_QX = 512;
constexpr size_t G_PS = G_QX + 4 * 4096;
constexpr size_t G_PX = G_PS + 32 * 8 * 16;
constexpr size_t G_SX = G_PX + 32 * 8 * 512;
constexpr size_t G_HX = G_SX + 4 * 64 * 64 * 8;
constexpr size_t G_MX = G_HX + 4 * 2048;
constexpr size_t G_END = G_MX + 4 * 64 * 32 * 8;
constexpr size_t G_SHADOW = 524288;
static_assert(G_END <= G_SHADOW, "ws");

constexpr int WO_LD = 1040;
constexpr int L_OM = 0;
constexpr int L_XS = 16384, L_YS = L_XS + 8192;
constexpr int L_RED = 32768, L_REDH = L_RED + 1024, L_QS = L_REDH + 1024, L_SM = L_QS + 1024, L_SL = L_SM + 128,
              L_GATE = L_SL + 128, L_FA = L_GATE + 64, L_FB = L_FA + 16, L_FC = L_FB + 16, L_FD = L_FC + 16,
              L_FQ = L_FD + 16, L_TAG = L_FQ + 16, L_FO = L_TAG + 16, L_OV = L_FA + 128, L_WO = L_OV + 8 * 256, L_END = L_WO + 64 * WO_LD;
constexpr int LDS_BYTES = 108 * 1024;
static_assert(L_END <= LDS_BYTES && L_WO % 16 == 0 && L_OV % 16 == 0 && L_FA % 16 == 0, "lds");

struct Params {
  const u16* x; const u16* k; const u16* v; const u16* wq; const u16* wo;
  const u16* w1; const u16* b1; const u16* w2; const u16* b2;
  u16* out; u64* ws;
};

__device__ __forceinline__ float asf(unsigned u) { return __builtin_bit_cast(float, u); }
__device__ __forceinline__ unsigned asu(float f) { return __builtin_bit_cast(unsigned, f); }
__device__ __forceinline__ float bflo(unsigned u) { return asf(u << 16); }
__device__ __forceinline__ float bfhi(unsigned u) { return asf(u & 0xffff0000u); }
__device__ __forceinline__ unsigned f2bf(float f) { return (unsigned)__builtin_bit_cast(u16, (__bf16)f); }
__device__ __forceinline__ float rbf(float f) { return (float)(__bf16)f; }
__device__ __forceinline__ unsigned cvtpk(float lo, float hi) {
  typedef __bf16 bf2 __attribute__((ext_vector_type(2)));
  bf2 r; r.x = (__bf16)lo; r.y = (__bf16)hi;
  return __builtin_bit_cast(unsigned, r);
}
template <int CTRL> __device__ __forceinline__ float dpp(float x) {
  return __builtin_bit_cast(float, __builtin_amdgcn_mov_dpp(__builtin_bit_cast(int, x), CTRL, 0xf, 0xf, true));
}
constexpr int XOR1 = 0xB1, XOR2 = 0x4E, HMIR = 0x141, MIR = 0x140, ROR8 = 0x128, ROR4 = 0x124;
__device__ __forceinline__ float tr32(float a, float b) {
  auto r = __builtin_amdgcn_permlane32_swap(asu(a), asu(b), false, false);
  return asf(r[0]) + asf(r[1]);
}
__device__ __forceinline__ float tr16(float a, float b) {
  auto r = __builtin_amdgcn_permlane16_swap(asu(a), asu(b), false, false);
  return asf(r[0]) + asf(r[1]);
}
__device__ __forceinline__ float max_rows(float x) {
  auto s = __builtin_amdgcn_permlane16_swap(asu(x), asu(x), false, false);
  x = fmaxf(asf(s[0]), asf(s[1]));
  auto t = __builtin_amdgcn_permlane32_swap(asu(x), asu(x), false, false);
  return fmaxf(asf(t[0]), asf(t[1]));
}
__device__ __forceinline__ float sum_rows(float x) { x = tr16(x, x); return tr32(x, x); }
__device__ __forceinline__ float wave_sum(float x) {
  x += dpp<XOR1>(x); x += dpp<XOR2>(x); x += dpp<HMIR>(x); x += dpp<MIR>(x);
  return sum_rows(x);
}
__device__ __forceinline__ float treduce8(const float (&v)[8], int lane) {
  const float u0 = tr32(v[0], v[4]), u1 = tr32(v[1], v[5]), u2 = tr32(v[2], v[6]), u3 = tr32(v[3], v[7]);
  const float w0 = tr16(u0, u2), w1 = tr16(u1, u3);
  const float t0 = w0 + dpp<ROR8>(w0), t1 = w1 + dpp<ROR8>(w1);
  return (lane & 8) ? t1 : t0;
}
__device__ __forceinline__ void ln_row(const float (&v)[8], float (&o)[8]) {
  float s = 0.f;
#pragma unroll
  for (int i = 0; i < 8; ++i) s += v[i];
  s = wave_sum(s);
  const float mean = s * (1.0f / DM);
  float ss = 0.f;
#pragma unroll
  for (int i = 0; i < 8; ++i) { const float d = v[i] - mean; ss = fmaf(d, d, ss); }
  ss = wave_sum(ss);
  const float var_b = rbf(ss * (1.0f / DM)), m_b = rbf(mean);
  const float den = rbf(sqrtf(rbf(var_b + 1e-5f)));
#pragma unroll
  for (int i = 0; i < 8; ++i) o[i] = rbf(rbf(v[i] - m_b) / den);
}
#define WG_BARRIER() asm volatile("s_waitcnt lgkmcnt(0)\n\ts_barrier" ::: "memory")
#define SCHED() __builtin_amdgcn_sched_barrier(0)
#define STAMP(k) do {} while (0)

struct Tag { unsigned lo8, hi; };
__device__ __forceinline__ void gstore(u64* p, unsigned payload24, Tag t) {
  __hip_atomic_store((GAS u64*)p, ((u64)t.hi << 32) | (u64)(payload24 | t.lo8), RLX_AGENT);
}
__device__ __forceinline__ u64 gload(const u64* p) { return __hip_atomic_load((const GAS u64*)p, RLX_AGENT); }
__device__ __forceinline__ bool gok(u64 g, Tag t) {
  return (unsigned)(g >> 32) == t.hi && ((unsigned)g & 0xff000000u) == t.lo8;
}
__device__ __forceinline__ unsigned gbad2(v4u g, Tag t) {
  return (((g.x ^ t.lo8) | (g.z ^ t.lo8)) & 0xff000000u) | (g.y ^ t.hi) | (g.w ^ t.hi);
}
__device__ __forceinline__ bool gok2(v4u g, Tag t) { return gbad2(g, t) == 0u; }
__device__ __forceinline__ unsigned enc24(float f) { unsigned u = asu(f); u += 0x7fu + ((u >> 8) & 1u); return u >> 8; }
__device__ __forceinline__ float dec24(u64 g) { return asf((unsigned)g << 8); }

__device__ __forceinline__ f32x4 mfma(v4u a, v4u b, f32x4 c) {
  return __builtin_amdgcn_mfma_f32_16x16x32_bf16(__builtin_bit_cast(bf16x8, a), __builtin_bit_cast(bf16x8, b), c, 0, 0, 0);
}

__device__ __forceinline__ float gemv16(const unsigned (&wv)[16], const float* vec, int rg, int lane) {
  float acc[8];
#pragma unroll
  for (int bb = 0; bb < 4; ++bb) {
    float a0 = 0.f, a1 = 0.f;
#pragma unroll
    for (int q4 = 0; q4 < 4; ++q4) {
      const f32x4 x0 = *(const f32x4*)(vec + bb * DM + 16 * rg + 4 * q4);
#pragma unroll
      for (int r = 0; r < 4; ++r) { a0 = fmaf(x0[r], bflo(wv[4 * q4 + r]), a0); a1 = fmaf(x0[r], bfhi(wv[4 * q4 + r]), a1); }
    }
    acc[bb * 2] = a0; acc[bb * 2 + 1] = a1;
  }
  return treduce8(acc, lane);
}
__device__ __forceinline__ void lds_set(unsigned addr, unsigned v) {
  asm volatile("s_waitcnt lgkmcnt(0)\n\tds_write_b32 %0, %1" :: "v"(addr), "v"(v) : "memory");
}
__device__ __forceinline__ void lds_wait4(unsigned addr, unsigned v) {
#pragma nounroll
  for (unsigned spin = 0; spin < (1u << 22); ++spin) {
    v4u f;
    asm volatile("ds_read_b128 %0, %1\n\ts_waitcnt lgkmcnt(0)" : "=v"(f) : "v"(addr) : "memory");
    if (__builtin_amdgcn_readfirstlane((f.x == v) & (f.y == v) & (f.z == v) & (f.w == v))) break;
    __builtin_amdgcn_s_sleep(1);
  }
}
__device__ __forceinline__ void lds_wait1(unsigned addr, unsigned v) {
#pragma nounroll
  for (unsigned spin = 0; spin < (1u << 22); ++spin) {
    unsigned f;
    asm volatile("ds_read_b32 %0, %1\n\ts_waitcnt lgkmcnt(0)" : "=v"(f) : "v"(addr) : "memory");
    if (__builtin_amdgcn_readfirstlane(f) == v) break;
    __builtin_amdgcn_s_sleep(1);
  }
}

struct Tile { v4u k[8]; v4u v[8]; };
__device__ __forceinline__ void ld8(v4u (&d)[8], const u16* base, bool valid, int off) {
  const __amdgpu_buffer_rsrc_t r = __builtin_amdgcn_make_buffer_rsrc((void*)base, 0, valid ? 8192 : 0, RSRC_W3);
#pragma unroll
  for (int i = 0; i < 8; ++i) d[i] = __builtin_amdgcn_raw_buffer_load_b128(r, off + (i & 3) * 1024, (i >> 2) * 4096, AUX_NT);
  SCHED();
}
__device__ __forceinline__ void qk_tile(v4u (&k)[8], v4u qf, float (&sc)[8], const u16* kn, bool nvalid, int koff, float bias) {
  constexpr float C2 = 0.08838834764831845f * 1.4426950408889634f;
  f32x4 d[8];
#pragma unroll
  for (int i = 0; i < 8; ++i) d[i] = mfma(k[i], qf, f32x4{0.f, 0.f, 0.f, 0.f});
  SCHED();
  ld8(k, kn, nvalid, koff);
#pragma unroll
  for (int i = 0; i < 8; ++i) sc[i] = fmaf((dpp<0x00>(d[i][0]) + dpp<0x55>(d[i][1])) + (dpp<0xAA>(d[i][2]) + dpp<0xFF>(d[i][3])), C2, bias);
}
__device__ __forceinline__ void pv_tile(v4u (&v)[8], const float (&sc)[8], float& m, float& l, f32x4 (&acc)[8], int lane,
                                        const u16* vn, bool nvalid, int voff, v4u (&k)[8], const u16* kn, int koff) {
  float tm = fmaxf(fmaxf(fmaxf(sc[0], sc[1]), fmaxf(sc[2], sc[3])), fmaxf(fmaxf(sc[4], sc[5]), fmaxf(sc[6], sc[7])));
  tm = max_rows(tm);
  if (__any(tm > m)) {
    const float mn = fmaxf(m, tm), al = __builtin_amdgcn_exp2f(m - mn);
    m = mn; l *= al;
    const float ah = __shfl(al, (lane >> 4) * 4);
#pragma unroll
    for (int j = 0; j < 8; ++j) acc[j] *= ah;
  }
  v4u pk;
#pragma unroll
  for (int k = 0; k < 4; ++k) {
    const float p0 = __builtin_amdgcn_exp2f(sc[2 * k] - m), p1 = __builtin_amdgcn_exp2f(sc[2 * k + 1] - m);
    l += p0 + p1; pk[k] = cvtpk(p0, p1);
  }
#pragma unroll
  for (int jj = 0; jj < 4; ++jj) {
    v4u lo, hi;
#pragma unroll
    for (int k2 = 0; k2 < 4; ++k2) {
      lo[k2] = __builtin_amdgcn_perm(v[2 * k2 + 1][jj], v[2 * k2][jj], 0x05040100u);
      hi[k2] = __builtin_amdgcn_perm(v[2 * k2 + 1][jj], v[2 * k2][jj], 0x07060302u);
    }
    acc[2 * jj] = mfma(pk, lo, acc[2 * jj]); acc[2 * jj + 1] = mfma(pk, hi, acc[2 * jj + 1]);
  }
  SCHED();
  ld8(v, vn, nvalid, voff);
}
template <int NK> __device__ __forceinline__ void poll_pairs(__amdgpu_buffer_rsrc_t r, int voff, int kstride, unsigned skip, Tag tag, v4u (&gv)[NK]) {
  int off[NK];
#pragma unroll
  for (int k = 0; k < NK; ++k) { off[k] = ((skip >> k) & 1u) ? 0x40000000 : voff; gv[k] = v4u{0u, 0u, 0u, 0u}; }
#pragma nounroll
  for (unsigned spin = 0; spin < SPIN_CAP; ++spin) {
    asm volatile("" ::: "memory");
    v4u t[NK];
#pragma unroll
    for (int k = 0; k < NK; ++k) t[k] = __builtin_amdgcn_raw_buffer_load_b128(r, off[k], k * kstride, AUX_SC1);
    bool pend = false;
#pragma unroll
    for (int k = 0; k < NK; ++k) {
      const bool got = gbad2(t[k], tag) == 0u;
      const bool live = off[k] == voff;
      if (got & live) { gv[k].x = t[k].x; gv[k].z = t[k].z; off[k] = 0x40000000; }
      pend |= live & !got;
    }
    if (!__any(pend)) break;
    __builtin_amdgcn_s_sleep(PSLEEP);
  }
}
template <int NK> __device__ __forceinline__ void gather8(const u64* src, int nparts, Tag tag, int lane, float& s0, float& s1) {
  const __amdgpu_buffer_rsrc_t r = __builtin_amdgcn_make_buffer_rsrc((void*)src, 0, NK * 1024, RSRC_W3);
  v4u gv[NK];
  unsigned skip = 0u;
#pragma unroll
  for (int k = 0; k < NK; ++k) skip |= (16 * k + (lane >> 2) >= nparts) ? (1u << k) : 0u;
  poll_pairs<NK>(r, lane * 16, 1024, skip, tag, gv);
  s0 = 0.f; s1 = 0.f;
#pragma unroll
  for (int k = 0; k < NK; ++k) { s0 += asf(gv[k].x << 8); s1 += asf(gv[k].z << 8); }
  s0 += dpp<ROR4>(s0); s1 += dpp<ROR4>(s1);
  s0 += dpp<ROR8>(s0); s1 += dpp<ROR8>(s1);
  s0 = sum_rows(s0); s1 = sum_rows(s1);
}

__global__ void __launch_bounds__(NTHR, 2) fused_decode(Params p) {
  __shared__ __attribute__((aligned(16))) unsigned char lds[LDS_BYTES];
  const int tid = threadIdx.x, lane = tid & 63, w = __builtin_amdgcn_readfirstlane(tid >> 6);
  const int bid = blockIdx.x, g = bid & 7, j = bid >> 3, b = j >> 3, s = j & 7, pair = b * 8 + g, cb = g * 8 + s;
  const int kbt = b, kg = g, ks = s, tile0 = s * 64, ntl = 64;
  const int kpair = kbt * 8 + kg, nmine = (ntl - w + 7) >> 3;
  u64* ws = p.ws;
  const unsigned lb = (unsigned)(size_t)(__attribute__((address_space(3))) unsigned char*)lds;
  STAMP(0);

  if (tid < 32) ((unsigned*)(lds + L_FA))[tid] = 0u;
  const size_t kvoff = ((size_t)kpair * SK + (tile0 + w) * 32) * 128;
  const u16* kb = p.k + kvoff;
  const u16* vb = p.v + kvoff;
  const int koff = ((lane & 15) >> 2) * 256 + (lane & 3) * 16 + (lane >> 4) * 64, voff = lane * 16;
  constexpr unsigned ltag = 1u;
  Tile A, B;
#define ISSUE_WINDOW(on) do { ld8(A.k, kb, on, koff); ld8(A.v, vb, on, voff); ld8(B.k, kb + 32768, on, koff); ld8(B.v, vb + 32768, on, voff); } while (0)
  const int cp = tid & 7, rg = tid >> 3;
  const bool hwg = j >= 18;
  const int f0 = 224 * g + 16 * (hwg ? j - 18 : 0);
  const int ob = lane >> 4, ocol = 2 * (lane & 7) + ((lane >> 3) & 1);
  u64 e_raw; v4u xr; unsigned wqv[16], w1v[16];
  int wA = w, wB = w, wC = w;
  asm volatile("" : "+s"(wA)); asm volatile("" : "+s"(wB)); asm volatile("" : "+s"(wC));
  WG_BARRIER();
  if (wA < 4) {
    e_raw = gload(ws + G_EPOCH);
    xr = *(const GAS v4u*)(p.x + w * DM + 8 * lane);
    const __amdgpu_buffer_rsrc_t rq = __builtin_amdgcn_make_buffer_rsrc((void*)(p.wq + g * 512 + 16 * j), 0, 512 * 8192 - 8192 + 32, RSRC_W3);
#pragma unroll
    for (int r = 0; r < 16; ++r) wqv[r] = __builtin_amdgcn_raw_buffer_load_b32(rq, rg * (16 * 8192) + cp * 4, r * 8192, AUX_W);
    const __amdgpu_buffer_rsrc_t r1 = __builtin_amdgcn_make_buffer_rsrc((void*)(p.w1 + f0), 0, hwg ? 511 * FF * 2 + 32 : 0, RSRC_W3);
#pragma unroll
    for (int r = 0; r < 16; ++r) w1v[r] = __builtin_amdgcn_raw_buffer_load_b32(r1, rg * (16 * FF * 2) + cp * 4, r * FF * 2, AUX_W);
    SCHED();
  }
  if (wC < 4) {
    const unsigned b1v = p.b1[f0 + ocol];
    SCHED();
    float* xs = (float*)(lds + L_XS);
    float* ys = (float*)(lds + L_YS);
    float* red = (float*)(lds + L_RED);
    float* redh = (float*)(lds + L_REDH);
    float xv[8], xn[8];
#pragma unroll
    for (int i = 0; i < 4; ++i) { xv[2 * i] = bflo(xr[i]); xv[2 * i + 1] = bfhi(xr[i]); }
    ln_row(xv, xn);
    *(f32x4*)(xs + w * DM + 8 * lane) = f32x4{xn[0], xn[1], xn[2], xn[3]};
    *(f32x4*)(xs + w * DM + 8 * lane + 4) = f32x4{xn[4], xn[5], xn[6], xn[7]};
    const u64 e_ = ((u64)__builtin_amdgcn_readfirstlane((unsigned)(e_raw >> 32)) << 32) | (u64)__builtin_amdgcn_readfirstlane((unsigned)e_raw);
    const u64 tag40 = ((e_ >> 24) * 0x5DEECE66DULL + 0xBULL) & ((1ULL << 40) - 1);
    Tag tag; tag.lo8 = (unsigned)(tag40 & 0xff) << 24; tag.hi = (unsigned)(tag40 >> 8);
    lds_set(lb + L_FA + 4 * w, ltag);
    STAMP(1);
    lds_wait4(lb + L_FA, ltag);
    red[w * 64 + lane] = gemv16(wqv, xs, rg, lane);
    lds_set(lb + L_FB + 4 * w, ltag);
    const __amdgpu_buffer_rsrc_t rqx = __builtin_amdgcn_make_buffer_rsrc((void*)(ws + G_QX + kbt * 4096 + kg * 512), 0, 4096, RSRC_W3);
    v4u gq[4];
    if (w == 0) {
      lds_wait4(lb + L_FB, ltag);
      const float q = (red[lane] + red[64 + lane]) + (red[128 + lane] + red[192 + lane]);
      gstore(ws + G_QX + ob * 4096 + g * 512 + 16 * j + ocol, f2bf(q), tag);
      STAMP(2);
    }
    if (hwg) {
      float y[8];
      ln_row(xn, y);
      *(f32x4*)(ys + w * DM + 8 * lane) = f32x4{y[0], y[1], y[2], y[3]};
      *(f32x4*)(ys + w * DM + 8 * lane + 4) = f32x4{y[4], y[5], y[6], y[7]};
      lds_set(lb + L_FC + 4 * w, ltag);
      lds_wait4(lb + L_FC, ltag);
      redh[w * 64 + lane] = gemv16(w1v, ys, rg, lane);
      lds_set(lb + L_FD + 4 * w, ltag);
      if (w == 1) {
        lds_wait4(lb + L_FD, ltag);
        const float hp = (redh[lane] + redh[64 + lane]) + (redh[128 + lane] + redh[192 + lane]);
        const float pre = rbf(rbf(hp) + bflo(b1v));
        const float hv = 0.5f * pre * (1.0f + tanhf(0.7978845608028654f * (pre + 0.044715f * pre * pre * pre)));
        gstore(ws + G_HX + ob * 2048 + f0 + ocol, f2bf(hv), tag);
      }
    }
    if (w == 0) {
      poll_pairs<4>(rqx, lane * 16, 1024, 0u, tag, gq);
#pragma unroll
      for (int k = 0; k < 4; ++k) ((unsigned*)(lds + L_QS))[64 * k + lane] = (gq[k].x & 0xffffu) | (gq[k].z << 16);
      *(v2u*)(lds + L_TAG) = v2u{tag.lo8, tag.hi};
      lds_set(lb + L_FQ, ltag);
      STAMP(3);
    }
  }
  unsigned pt[4];
  {
    const size_t ro = ((size_t)kpair * SK + tile0 * 32) * 128;
    const __amdgpu_buffer_rsrc_t rk_ = __builtin_amdgcn_make_buffer_rsrc((void*)(p.k + ro), 0, w == 7 ? ntl * 8192 : 0, RSRC_W3);
    const __amdgpu_buffer_rsrc_t rv_ = __builtin_amdgcn_make_buffer_rsrc((void*)(p.v + ro), 0, w == 7 ? ntl * 8192 : 0, RSRC_W3);
    pt[0] = __builtin_amdgcn_raw_buffer_load_b32(rk_, lane * PRET, 0, AUX_NT);
    pt[1] = __builtin_amdgcn_raw_buffer_load_b32(rv_, lane * PRET, 0, AUX_NT);
    pt[2] = __builtin_amdgcn_raw_buffer_load_b32(rk_, (lane + 64) * PRET, 0, AUX_NT);
    pt[3] = __builtin_amdgcn_raw_buffer_load_b32(rv_, (lane + 64) * PRET, 0, AUX_NT);
  }
  SCHED();
  ISSUE_WINDOW(true);
  asm volatile("" :: "v"(pt[0]), "v"(pt[1]), "v"(pt[2]), "v"(pt[3]));
  lds_wait1(lb + L_FQ, ltag);
  Tag tag;
  { const v2u t = *(const v2u*)(lds + L_TAG); tag.lo8 = __builtin_amdgcn_readfirstlane(t.x); tag.hi = __builtin_amdgcn_readfirstlane(t.y); }
  STAMP(4);
  const v4u qf = *(const v4u*)(lds + L_QS + ((lane & 15) >> 2) * 256 + ((lane & 3) + 4 * (lane >> 4)) * 16);

  float m = -INFINITY, lsum = 0.f;
  f32x4 acc[8];
#pragma unroll
  for (int i = 0; i < 8; ++i) acc[i] = f32x4{0.f, 0.f, 0.f, 0.f};
  float sA[8], sB[8];
#pragma nounroll
  for (int t = 0; t < nmine; t += 2) {
    const bool na = t + 2 < nmine, nb = t + 3 < nmine;
    const size_t oa = (size_t)(na ? t + 2 : 0) * 32768, ob2 = (size_t)(nb ? t + 3 : 0) * 32768;
    qk_tile(A.k, qf, sA, kb + oa, na, koff, 0.f);
    pv_tile(A.v, sA, m, lsum, acc, lane, vb + oa, na, voff, A.k, kb + oa, koff);
    qk_tile(B.k, qf, sB, kb + ob2, nb, koff, t + 1 < nmine ? 0.f : -INFINITY);
    pv_tile(B.v, sB, m, lsum, acc, lane, vb + ob2, nb, voff, B.k, kb + ob2, koff);
  }
  lsum = sum_rows(lsum);
  STAMP(5);
  float* om = (float*)(lds + L_OM);
  float* sm = (float*)(lds + L_SM);
  float* sl = (float*)(lds + L_SL);
  *(f32x4*)(om + w * 512 + lane * 8) = f32x4{acc[0][0], acc[1][0], acc[2][0], acc[3][0]};
  *(f32x4*)(om + w * 512 + lane * 8 + 4) = f32x4{acc[4][0], acc[5][0], acc[6][0], acc[7][0]};
  if (lane < 16 && (lane & 3) == 0) { sm[(lane >> 2) * 8 + w] = m; sl[(lane >> 2) * 8 + w] = lsum; }
  const int r8 = lane >> 3, c8 = lane & 7;

  v4u bw[2][8];
  if (w < 4) {
    v4u t16[16];
    const __amdgpu_buffer_rsrc_t rwo = __builtin_amdgcn_make_buffer_rsrc((void*)(p.wo + (size_t)(g * 512 + 64 * s) * DM + 128 * w), 0, ((ABL_RED & 1) || ((ABL_RED & 8) && w != b)) ? 0 : 63 * 1024 + 256, RSRC_W3);
#pragma unroll
    for (int r = 0; r < 16; ++r) t16[r] = __builtin_amdgcn_raw_buffer_load_b128(rwo, (lane >> 4) * 1024 + (lane & 15) * 16, r * 4096, AUX_W2);
    v4u w2v[8];
    const int f0 = 256 * g + 64 * w;
    const __amdgpu_buffer_rsrc_t rw2 = __builtin_amdgcn_make_buffer_rsrc((void*)(p.w2 + (size_t)(g < 7 ? f0 : 0) * DM + 64 * s), 0, (g < 7 && !(ABL_RED & 2) && !((ABL_RED & 16) && w != b)) ? 63 * 1024 + 128 : 0, RSRC_W3);
#pragma unroll
    for (int i = 0; i < 8; ++i) w2v[i] = __builtin_amdgcn_raw_buffer_load_b128(rw2, r8 * 1024 + c8 * 16, i * 8192, AUX_W2);
    const u64* hsrc = ws + G_HX + b * 2048 + f0 + lane;
    u64 hg = gload(hsrc);
    SCHED();
#pragma unroll
    for (int ks = 0; ks < 2; ++ks)
#pragma unroll
      for (int jj = 0; jj < 4; ++jj)
#pragma unroll
        for (int k2 = 0; k2 < 4; ++k2) {
          bw[ks][2 * jj][k2] = __builtin_amdgcn_perm(t16[8 * ks + 2 * k2 + 1][jj], t16[8 * ks + 2 * k2][jj], 0x05040100u);
          bw[ks][2 * jj + 1][k2] = __builtin_amdgcn_perm(t16[8 * ks + 2 * k2 + 1][jj], t16[8 * ks + 2 * k2][jj], 0x07060302u);
        }
    if (g < 7 && !(ABL_RED & 4)) {
      if (!__all(gok(hg, tag))) {
        unsigned spin = 0;
        do { __builtin_amdgcn_s_sleep(PSLEEP); hg = gload(hsrc); } while (!__all(gok(hg, tag)) && ++spin < SPIN_CAP);
      }
      const float hl = bflo((unsigned)hg);
      float ma[8];
#pragma unroll
      for (int e = 0; e < 8; ++e) ma[e] = 0.f;
#pragma unroll
      for (int i = 0; i < 8; ++i) {
        const float hv = __shfl(hl, 8 * i + r8);
#pragma unroll
        for (int e = 0; e < 4; ++e) { ma[2 * e] = fmaf(hv, bflo(w2v[i][e]), ma[2 * e]); ma[2 * e + 1] = fmaf(hv, bfhi(w2v[i][e]), ma[2 * e + 1]); }
      }
      const float r = treduce8(ma, lane);
      gstore(ws + G_MX + ((size_t)(b * 64 + 8 * s + c8) * 32 + 4 * g + w) * 8 + r8, enc24(r), tag);
    }
  }
  STAMP(6);
  const unsigned xp = *(const GAS unsigned*)(p.x + b * DM + cb * 8 + 2 * (lane & 3));
  WG_BARRIER();
  {
    const int h = tid >> 7;
    const f32x4 m0 = *(const f32x4*)(sm + h * 8), m1 = *(const f32x4*)(sm + h * 8 + 4);
    float ov[8];
#pragma unroll
    for (int ww = 0; ww < 8; ++ww) ov[ww] = om[ww * 512 + tid];
    const f32x4 l0 = *(const f32x4*)(sl + h * 8), l1 = *(const f32x4*)(sl + h * 8 + 4);
    SCHED();
    const float M = fmaxf(fmaxf(fmaxf(m0[0], m0[1]), fmaxf(m0[2], m0[3])), fmaxf(fmaxf(m1[0], m1[1]), fmaxf(m1[2], m1[3])));
    float O = 0.f, L = 0.f;
#pragma unroll
    for (int ww = 0; ww < 4; ++ww) { const float f = __builtin_amdgcn_exp2f(m0[ww] - M); O = fmaf(f, ov[ww], O); L = fmaf(f, l0[ww], L); }
#pragma unroll
    for (int ww = 0; ww < 4; ++ww) { const float f = __builtin_amdgcn_exp2f(m1[ww] - M); O = fmaf(f, ov[4 + ww], O); L = fmaf(f, l1[ww], L); }
    gstore(ws + G_PX + (size_t)(kpair * 8 + ks) * 512 + tid, enc24(O), tag);
    const int d = tid & 127;
    if (d < 4) {
      const unsigned u = (d & 2) ? asu(L) : asu(M);
      gstore(ws + G_PS + (size_t)((kpair * 4 + h) * 8 + ks) * 4 + d, (d & 1) ? (u & 0xffffu) : (u >> 16), tag);
    }
  }
  STAMP(7);
  if (ABL_T & 2) {} else if (w == 4) {
    const __amdgpu_buffer_rsrc_t rp = __builtin_amdgcn_make_buffer_rsrc((void*)(ws + G_PX + (size_t)pair * 4096 + 64 * s), 0, 8 * 4096, RSRC_W3);
    const __amdgpu_buffer_rsrc_t rs = __builtin_amdgcn_make_buffer_rsrc((void*)(ws + G_PS + (size_t)(pair * 4 + (s >> 1)) * 32), 0, 256, RSRC_W3);
    const int poff = (lane >> 5) * 4096 + (lane & 31) * 16;
    v4u gp[4]; v2u gs;
#pragma nounroll
    for (unsigned spin = 0; spin < SPIN_CAP; ++spin) {
      asm volatile("" ::: "memory");
      gs = __builtin_amdgcn_raw_buffer_load_b64(rs, (lane & 31) * 8, 0, AUX_SC1);
#pragma unroll
      for (int k = 0; k < 4; ++k) gp[k] = __builtin_amdgcn_raw_buffer_load_b128(rp, poff, k * 8192, AUX_SC1);
      const unsigned bad = gbad2(gp[0], tag) | gbad2(gp[1], tag) | gbad2(gp[2], tag) | gbad2(gp[3], tag) | (gs.y ^ tag.hi) | ((gs.x ^ tag.lo8) & 0xff000000u);
      if (__all(bad == 0u)) break;
      __builtin_amdgcn_s_sleep(PSLEEP);
    }
    STAMP(8);
    const unsigned sv = gs.x & 0xffffu;
    const float Mk = asf((asu(dpp<0x00>(asf(sv))) << 16) | asu(dpp<0x55>(asf(sv))));
    const float Lk = asf((asu(dpp<0xAA>(asf(sv))) << 16) | asu(dpp<0xFF>(asf(sv))));
    float M = fmaxf(Mk, dpp<ROR4>(Mk)); M = fmaxf(M, dpp<ROR8>(M));
    { auto t = __builtin_amdgcn_permlane16_swap(asu(M), asu(M), false, false); M = fmaxf(asf(t[0]), asf(t[1])); }
    const float fk = __builtin_amdgcn_exp2f(Mk - M);
    float L = fk * Lk; L += dpp<ROR4>(L); L += dpp<ROR8>(L); L = tr16(L, L);
    float o0 = 0.f, o1 = 0.f;
#pragma unroll
    for (int k = 0; k < 4; ++k) {
      const float fe = asf(__builtin_amdgcn_readlane(asu(fk), 8 * k)), fo = asf(__builtin_amdgcn_readlane(asu(fk), 8 * k + 4));
      const float f = (lane >> 5) ? fo : fe;
      o0 = fmaf(f, asf(gp[k].x << 8), o0); o1 = fmaf(f, asf(gp[k].z << 8), o1);
    }
    const float ot = tr32(o0, o1);
    const int e = 2 * (lane & 31) + (lane >> 5);
    ((u16*)(lds + L_OV))[(e & 32) + (e & 3) * 8 + ((e >> 2) & 7)] = (u16)f2bf(ot / L);
    lds_set(lb + L_FO, ltag);
  } else if (w < 4) {
    const __amdgpu_buffer_rsrc_t rsx = __builtin_amdgcn_make_buffer_rsrc((void*)(ws + G_SX + ((size_t)(b * 64 + 16 * w) * 64 + cb) * 8), 0, 16 * 4096, RSRC_W3);
    lds_wait1(lb + L_FO, ltag);
    const v4u a0 = *(const v4u*)(lds + L_OV + (lane >> 4) * 16), a1 = *(const v4u*)(lds + L_OV + 64 + (lane >> 4) * 16);
    f32x4 d[8];
#pragma unroll
    for (int jx = 0; jx < 8; ++jx) d[jx] = mfma(a0, bw[0][jx], f32x4{0.f, 0.f, 0.f, 0.f});
#pragma unroll
    for (int jx = 0; jx < 8; ++jx) d[jx] = mfma(a1, bw[1][jx], d[jx]);
    const int g4 = lane >> 4;
    const float v0 = (g4 & 2) ? ((g4 & 1) ? d[6][0] : d[4][0]) : ((g4 & 1) ? d[2][0] : d[0][0]);
    const float v1 = (g4 & 2) ? ((g4 & 1) ? d[7][0] : d[5][0]) : ((g4 & 1) ? d[3][0] : d[1][0]);
    __builtin_amdgcn_raw_buffer_store_b128(v4u{enc24(v0) | tag.lo8, tag.hi, enc24(v1) | tag.lo8, tag.hi}, rsx, (lane & 15) * 4096 + g4 * 16, 0, AUX_SC1);
    STAMP(9);
  } else if (w == 5 && !(ABL_T & 1)) {
    const unsigned b2p = *(const GAS unsigned*)(p.b2 + cb * 8 + 2 * (lane & 3));
    float m0, m1;
    gather8<2>(ws + G_MX + (size_t)(b * 64 + cb) * 256, 28, tag, lane, m0, m1);
    m0 = rbf(rbf(m0) + bflo(b2p)); m1 = rbf(rbf(m1) + bfhi(b2p));
    float2 gt;
    gt.x = rbf(m0 / (1.0f + __expf(-m0))); gt.y = rbf(m1 / (1.0f + __expf(-m1)));
    float s0, s1;
    gather8<4>(ws + G_SX + (size_t)(b * 64 + cb) * 512, 64, tag, lane, s0, s1);
    STAMP(10);
    const float o0 = bflo(xp) + rbf(rbf(s0) * gt.x), o1 = bfhi(xp) + rbf(rbf(s1) * gt.y);
    if (lane < 4) *(GAS unsigned*)(p.out + b * DM + cb * 8 + 2 * lane) = cvtpk(o0, o1);
    if (bid == 0 && lane == 0) __hip_atomic_store((GAS u64*)(ws + G_EPOCH), ((u64)tag.hi << 32) | tag.lo8, RLX_AGENT);
  }
}

}

extern "C" void kernel_launch(void* const* d_in, const int* in_sizes, int n_in,
                              void* d_out, int out_size, void* d_ws, size_t ws_size,
                              hipStream_t stream) {
  Params p;
  p.x = (const u16*)d_in[0]; p.k = (const u16*)d_in[1]; p.v = (const u16*)d_in[2];
  p.wq = (const u16*)d_in[3]; p.wo = (const u16*)d_in[4];
  p.w1 = (const u16*)d_in[6]; p.b1 = (const u16*)d_in[7]; p.w2 = (const u16*)d_in[8]; p.b2 = (const u16*)d_in[9];
  p.out = (u16*)d_out; p.ws = (u64*)d_ws;
  hipLaunchKernelGGL(fused_decode, dim3(NWG), dim3(NTHR), 0, stream, p);
}
```

```cpp
#define STAMPS
#include <cstdio>
#include <hip/hip_runtime.h>
#include <hip/hip_bf16.h>
#include <stdint.h>

namespace {

typedef unsigned v4u __attribute__((ext_vector_type(4)));
typedef unsigned v2u __attribute__((ext_vector_type(2)));
typedef float f32x4 __attribute__((ext_vector_type(4)));
typedef short bf16x8 __attribute__((ext_vector_type(8)));
typedef unsigned long long u64;
typedef unsigned short u16;
#define GAS __attribute__((address_space(1)))
#define RLX_AGENT __ATOMIC_RELAXED, __HIP_MEMORY_SCOPE_AGENT

constexpr int DM = 512, SK = 16384, FF = 1792;
constexpr int NWG = 256, NTHR = 512;
constexpr int W = 32;
constexpr int RSRC_W3 = 0x00020000;
constexpr int AUX_NT = 2, AUX_SC1 = 16;
#define AUX_W 0
#define AUX_K 2
#define AUX_V 2
#define TOPWIN 0
#define PW 1
#define AUX_W2 0
#define LATEK 0
#define ABL_T 0
#define XMAP 2
#define SKT 0
#define PRET 0
#define LATESIDE 3
#define HSTR 4096
#define DUP1 0
#define RCP 0
#define ABL_RED 0
#define DRY 0
#define AUX_G 16
#define PSLEEP 2
constexpr unsigned SPIN_CAP = 1u << 16;

constexpr size_t G_EPOCH = 0;
constexpr size_t G_QX = 512;
constexpr size_t G_PS = G_QX + 4 * 4096;
constexpr size_t G_PX = G_PS + 32 * 8 * 16;
constexpr size_t G_SX = G_PX + 32 * 8 * 512;
constexpr size_t G_HX = G_SX + 4 * 64 * 64 * 8;
constexpr size_t G_MX = G_HX + 4 * 2048;
constexpr size_t G_END = G_MX + 4 * 64 * 32 * 8;
constexpr size_t G_SHADOW = 524288;
static_assert(G_END <= G_SHADOW, "ws");

constexpr int WO_LD = 1040;
constexpr int L_OM = 0;
constexpr int L_XS = 16384, L_YS = L_XS + 8192;
constexpr int L_RED = 32768, L_REDH = L_RED + 1024, L_QS = L_REDH + 1024, L_SM = L_QS + 1024, L_SL = L_SM + 128,
              L_GATE = L_SL + 128, L_FA = L_GATE + 64, L_FB = L_FA + 16, L_FC = L_FB + 16, L_FD = L_FC + 16,
              L_FQ = L_FD + 16, L_TAG = L_FQ + 16, L_FO = L_TAG + 16, L_FL = L_FO + 16, L_OV = L_FA + 128, L_WO = L_OV + 8 * 256, L_END = L_WO + 64 * WO_LD;
constexpr int LDS_BYTES = 108 * 1024;
static_assert(L_END <= LDS_BYTES && L_WO % 16 == 0 && L_OV % 16 == 0 && L_FA % 16 == 0, "lds");

struct Params {
  const u16* x; const u16* k; const u16* v; const u16* wq; const u16* wo;
  const u16* w1; const u16* b1; const u16* w2; const u16* b2;
  u16* out; u64* ws;
};

__device__ __forceinline__ float asf(unsigned u) { return __builtin_bit_cast(float, u); }
__device__ __forceinline__ unsigned asu(float f) { return __builtin_bit_cast(unsigned, f); }
__device__ __forceinline__ float bflo(unsigned u) { return asf(u << 16); }
__device__ __forceinline__ float bfhi(unsigned u) { return asf(u & 0xffff0000u); }
__device__ __forceinline__ unsigned f2bf(float f) { return (unsigned)__builtin_bit_cast(u16, (__bf16)f); }
__device__ __forceinline__ float rbf(float f) { return (float)(__bf16)f; }
__device__ __forceinline__ unsigned cvtpk(float lo, float hi) {
  typedef __bf16 bf2 __attribute__((ext_vector_type(2)));
  bf2 r; r.x = (__bf16)lo; r.y = (__bf16)hi;
  return __builtin_bit_cast(unsigned, r);
}
template <int CTRL> __device__ __forceinline__ float dpp(float x) {
  return __builtin_bit_cast(float, __builtin_amdgcn_mov_dpp(__builtin_bit_cast(int, x), CTRL, 0xf, 0xf, true));
}
constexpr int XOR1 = 0xB1, XOR2 = 0x4E, HMIR = 0x141, MIR = 0x140, ROR8 = 0x128, ROR4 = 0x124;
__device__ __forceinline__ float tr32(float a, float b) {
  auto r = __builtin_amdgcn_permlane32_swap(asu(a), asu(b), false, false);
  return asf(r[0]) + asf(r[1]);
}
__device__ __forceinline__ float tr16(float a, float b) {
  auto r = __builtin_amdgcn_permlane16_swap(asu(a), asu(b), false, false);
  return asf(r[0]) + asf(r[1]);
}
__device__ __forceinline__ float max_rows(float x) {
  auto s = __builtin_amdgcn_permlane16_swap(asu(x), asu(x), false, false);
  x = fmaxf(asf(s[0]), asf(s[1]));
  auto t = __builtin_amdgcn_permlane32_swap(asu(x), asu(x), false, false);
  return fmaxf(asf(t[0]), asf(t[1]));
}
__device__ __forceinline__ float sum_rows(float x) { x = tr16(x, x); return tr32(x, x); }
__device__ __forceinline__ float wave_sum(float x) {
  x += dpp<XOR1>(x); x += dpp<XOR2>(x); x += dpp<HMIR>(x); x += dpp<MIR>(x);
  return sum_rows(x);
}
__device__ __forceinline__ float treduce8(const float (&v)[8], int lane) {
  const float u0 = tr32(v[0], v[4]), u1 = tr32(v[1], v[5]), u2 = tr32(v[2], v[6]), u3 = tr32(v[3], v[7]);
  const float w0 = tr16(u0, u2), w1 = tr16(u1, u3);
  const float t0 = w0 + dpp<ROR8>(w0), t1 = w1 + dpp<ROR8>(w1);
  return (lane & 8) ? t1 : t0;
}
__device__ __forceinline__ void ln_row(const float (&v)[8], float (&o)[8]) {
  float s = 0.f;
#pragma unroll
  for (int i = 0; i < 8; ++i) s += v[i];
  s = wave_sum(s);
  const float mean = s * (1.0f / DM);
  float ss = 0.f;
#pragma unroll
  for (int i = 0; i < 8; ++i) { const float d = v[i] - mean; ss = fmaf(d, d, ss); }
  ss = wave_sum(ss);
  const float var_b = rbf(ss * (1.0f / DM)), m_b = rbf(mean);
  const float den = rbf(sqrtf(rbf(var_b + 1e-5f)));
#pragma unroll
  for (int i = 0; i < 8; ++i) o[i] = rbf(rbf(v[i] - m_b) / den);
}
#define WG_BARRIER() asm volatile("s_waitcnt lgkmcnt(0)\n\ts_barrier" ::: "memory")
#define SCHED() __builtin_amdgcn_sched_barrier(0)
#define NSTAMP 12
#define STAMP_OFF (8u << 20)
#define STAMP(k) do { SCHED(); stamp[k] = __builtin_amdgcn_s_memrealtime(); SCHED(); } while (0)

struct Tag { unsigned lo8, hi; };
__device__ __forceinline__ void gstore(u64* p, unsigned payload24, Tag t) {
  __hip_atomic_store((GAS u64*)p, ((u64)t.hi << 32) | (u64)(payload24 | t.lo8), RLX_AGENT);
}
__device__ __forceinline__ u64 gload(const u64* p) { return __hip_atomic_load((const GAS u64*)p, RLX_AGENT); }
__device__ __forceinline__ bool gok(u64 g, Tag t) {
  return (unsigned)(g >> 32) == t.hi && ((unsigned)g & 0xff000000u) == t.lo8;
}
__device__ __forceinline__ unsigned gbad2(v4u g, Tag t) {
  return (((g.x ^ t.lo8) | (g.z ^ t.lo8)) & 0xff000000u) | (g.y ^ t.hi) | (g.w ^ t.hi);
}
__device__ __forceinline__ bool gok2(v4u g, Tag t) { return gbad2(g, t) == 0u; }
__device__ __forceinline__ unsigned enc24(float f) { unsigned u = asu(f); u += 0x7fu + ((u >> 8) & 1u); return u >> 8; }
__device__ __forceinline__ float dec24(u64 g) { return asf((unsigned)g << 8); }

__device__ __forceinline__ f32x4 mfma(v4u a, v4u b, f32x4 c) {
  return __builtin_amdgcn_mfma_f32_16x16x32_bf16(__builtin_bit_cast(bf16x8, a), __builtin_bit_cast(bf16x8, b), c, 0, 0, 0);
}

__device__ __forceinline__ float gemv16(const unsigned (&wv)[16], const float* vec, int rg, int lane) {
  float acc[8];
#pragma unroll
  for (int bb = 0; bb < 4; ++bb) {
    float a0 = 0.f, a1 = 0.f;
#pragma unroll
    for (int q4 = 0; q4 < 4; ++q4) {
      const f32x4 x0 = *(const f32x4*)(vec + bb * DM + 16 * rg + 4 * q4);
#pragma unroll
      for (int r = 0; r < 4; ++r) { a0 = fmaf(x0[r], bflo(wv[4 * q4 + r]), a0); a1 = fmaf(x0[r], bfhi(wv[4 * q4 + r]), a1); }
    }
    acc[bb * 2] = a0; acc[bb * 2 + 1] = a1;
  }
  return treduce8(acc, lane);
}
__device__ __forceinline__ void lds_set(unsigned addr, unsigned v) {
  asm volatile("s_waitcnt lgkmcnt(0)\n\tds_write_b32 %0, %1" :: "v"(addr), "v"(v) : "memory");
}
__device__ __forceinline__ void lds_wait4(unsigned addr, unsigned v) {
#pragma nounroll
  for (unsigned spin = 0; spin < (1u << 22); ++spin) {
    v4u f;
    asm volatile("ds_read_b128 %0, %1\n\ts_waitcnt lgkmcnt(0)" : "=v"(f) : "v"(addr) : "memory");
    if (__builtin_amdgcn_readfirstlane((f.x == v) & (f.y == v) & (f.z == v) & (f.w == v))) break;
    __builtin_amdgcn_s_sleep(1);
  }
}
__device__ __forceinline__ void lds_wait1(unsigned addr, unsigned v) {
#pragma nounroll
  for (unsigned spin = 0; spin < (1u << 22); ++spin) {
    unsigned f;
    asm volatile("ds_read_b32 %0, %1\n\ts_waitcnt lgkmcnt(0)" : "=v"(f) : "v"(addr) : "memory");
    if (__builtin_amdgcn_readfirstlane(f) == v) break;
    __builtin_amdgcn_s_sleep(1);
  }
}

struct Tile { v4u k[8]; v4u v[8]; };
__device__ __forceinline__ void ld8(v4u (&d)[8], const u16* base, bool valid, int off) {
  const __amdgpu_buffer_rsrc_t r = __builtin_amdgcn_make_buffer_rsrc((void*)base, 0, valid ? HSTR + 4096 : 0, RSRC_W3);
#pragma unroll
  for (int i = 0; i < 8; ++i) d[i] = __builtin_amdgcn_raw_buffer_load_b128(r, off + (i & 3) * 1024, (i >> 2) * HSTR, AUX_NT);
  SCHED();
}
__device__ __forceinline__ void qk_tile(v4u (&k)[8], v4u qf, float (&sc)[8], const u16* kn, bool nvalid, int koff, float bias) {
  constexpr float C2 = 0.08838834764831845f * 1.4426950408889634f;
  f32x4 d[8];
#pragma unroll
  for (int i = 0; i < 8; ++i) d[i] = mfma(k[i], qf, f32x4{0.f, 0.f, 0.f, 0.f});
  SCHED();
  ld8(k, kn, nvalid, koff);
#pragma unroll
  for (int i = 0; i < 8; ++i) sc[i] = fmaf((dpp<0x00>(d[i][0]) + dpp<0x55>(d[i][1])) + (dpp<0xAA>(d[i][2]) + dpp<0xFF>(d[i][3])), C2, bias);
}
__device__ __forceinline__ void pv_tile(v4u (&v)[8], const float (&sc)[8], float& m, float& l, f32x4 (&acc)[8], int lane,
                                        const u16* vn, bool nvalid, int voff, v4u (&k)[8], const u16* kn, int koff) {
  float tm = fmaxf(fmaxf(fmaxf(sc[0], sc[1]), fmaxf(sc[2], sc[3])), fmaxf(fmaxf(sc[4], sc[5]), fmaxf(sc[6], sc[7])));
  tm = max_rows(tm);
  if (__any(tm > m)) {
    const float mn = fmaxf(m, tm), al = __builtin_amdgcn_exp2f(m - mn);
    m = mn; l *= al;
    const float ah = __shfl(al, (lane >> 4) * 4);
#pragma unroll
    for (int j = 0; j < 8; ++j) acc[j] *= ah;
  }
  v4u pk;
#pragma unroll
  for (int k = 0; k < 4; ++k) {
    const float p0 = __builtin_amdgcn_exp2f(sc[2 * k] - m), p1 = __builtin_amdgcn_exp2f(sc[2 * k + 1] - m);
    l += p0 + p1; pk[k] = cvtpk(p0, p1);
  }
#pragma unroll
  for (int jj = 0; jj < 4; ++jj) {
    v4u lo, hi;
#pragma unroll
    for (int k2 = 0; k2 < 4; ++k2) {
      lo[k2] = __builtin_amdgcn_perm(v[2 * k2 + 1][jj], v[2 * k2][jj], 0x05040100u);
      hi[k2] = __builtin_amdgcn_perm(v[2 * k2 + 1][jj], v[2 * k2][jj], 0x07060302u);
    }
    acc[2 * jj] = mfma(pk, lo, acc[2 * jj]); acc[2 * jj + 1] = mfma(pk, hi, acc[2 * jj + 1]);
  }
  SCHED();
  ld8(v, vn, nvalid, voff);
}
template <int NK> __device__ __forceinline__ void poll_pairs(__amdgpu_buffer_rsrc_t r, int voff, int kstride, unsigned skip, Tag tag, v4u (&gv)[NK]) {
  int off[NK];
#pragma unroll
  for (int k = 0; k < NK; ++k) { off[k] = ((skip >> k) & 1u) ? 0x40000000 : voff; gv[k] = v4u{0u, 0u, 0u, 0u}; }
#pragma nounroll
  for (unsigned spin = 0; spin < SPIN_CAP; ++spin) {
    asm volatile("" ::: "memory");
    v4u t[NK];
#pragma unroll
    for (int k = 0; k < NK; ++k) t[k] = __builtin_amdgcn_raw_buffer_load_b128(r, off[k], k * kstride, AUX_SC1);
    bool pend = false;
#pragma unroll
    for (int k = 0; k < NK; ++k) {
      const bool got = gbad2(t[k], tag) == 0u;
      const bool live = off[k] == voff;
      if (got & live) { gv[k].x = t[k].x; gv[k].z = t[k].z; off[k] = 0x40000000; }
      pend |= live & !got;
    }
    if (!__any(pend)) break;
    __builtin_amdgcn_s_sleep(PSLEEP);
  }
}
template <int NK> __device__ __forceinline__ void gather8(const u64* src, int nparts, Tag tag, int lane, float& s0, float& s1) {
  const __amdgpu_buffer_rsrc_t r = __builtin_amdgcn_make_buffer_rsrc((void*)src, 0, NK * 1024, RSRC_W3);
  v4u gv[NK];
  unsigned skip = 0u;
#pragma unroll
  for (int k = 0; k < NK; ++k) skip |= (16 * k + (lane >> 2) >= nparts) ? (1u << k) : 0u;
  poll_pairs<NK>(r, lane * 16, 1024, skip, tag, gv);
  s0 = 0.f; s1 = 0.f;
#pragma unroll
  for (int k = 0; k < NK; ++k) { s0 += asf(gv[k].x << 8); s1 += asf(gv[k].z << 8); }
  s0 += dpp<ROR4>(s0); s1 += dpp<ROR4>(s1);
  s0 += dpp<ROR8>(s0); s1 += dpp<ROR8>(s1);
  s0 = sum_rows(s0); s1 = sum_rows(s1);
}

__global__ void __launch_bounds__(NTHR, 2) fused_decode(Params p) {
  __shared__ __attribute__((aligned(16))) unsigned char lds[LDS_BYTES];
  const int tid = threadIdx.x, lane = tid & 63, w = __builtin_amdgcn_readfirstlane(tid >> 6);
  const int bid = blockIdx.x, g = bid & 7, j = bid >> 3, b = j >> 3, s = j & 7, pair = b * 8 + g, cb = g * 8 + s;
  const int kbt = g >> 1, kg = j >> 2, ks = (j & 3) * 2 + (g & 1);
  const int tile0 = (j & 3) * 128 + ((g & 1) ? 64 - SKT : 0), ntl = (g & 1) ? 64 + SKT : 64 - SKT;
  const int kpair = kbt * 8 + kg, nmine = (ntl - w + 7) >> 3;
  u64* ws = p.ws;
  const unsigned lb = (unsigned)(size_t)(__attribute__((address_space(3))) unsigned char*)lds;
  u64 stamp[NSTAMP];
  for (int k = 0; k < NSTAMP; ++k) stamp[k] = 0;
  STAMP(0);
  const u64 c0_ = __builtin_amdgcn_s_memtime();

  if (tid < 32) ((unsigned*)(lds + L_FA))[tid] = 0u;
  const size_t kvoff = ((size_t)kpair * SK + tile0 * 32 + w * (HSTR == 4096 ? 32 : 16)) * 128;
  const u16* kb = p.k + kvoff;
  const u16* vb = p.v + kvoff;
  const int koff = ((lane & 15) >> 2) * 256 + (lane & 3) * 16 + (lane >> 4) * 64, voff = lane * 16;
  constexpr unsigned ltag = 1u;
  Tile A, B;
#define ISSUE_WINDOW(on) do { ld8(A.k, kb, on, koff); ld8(A.v, vb, on, voff); ld8(B.k, kb + 32768, on, koff); ld8(B.v, vb + 32768, on, voff); } while (0)
  const int cp = tid & 7, rg = tid >> 3;
  const bool hwg = j >= 18;
  const int f0 = 224 * g + 16 * (hwg ? j - 18 : 0);
  const int ob = lane >> 4, ocol = 2 * (lane & 7) + ((lane >> 3) & 1);
  u64 e_raw; v4u xr; unsigned wqv[16], w1v[16];
  int wA = w, wB = w, wC = w;
  asm volatile("" : "+s"(wA)); asm volatile("" : "+s"(wB)); asm volatile("" : "+s"(wC));
  WG_BARRIER();
  if (wA < 4) {
    e_raw = gload(ws + G_EPOCH);
    xr = *(const GAS v4u*)(p.x + w * DM + 8 * lane);
    const __amdgpu_buffer_rsrc_t rq = __builtin_amdgcn_make_buffer_rsrc((void*)(p.wq + g * 512 + 16 * j), 0, 512 * 8192 - 8192 + 32, RSRC_W3);
#pragma unroll
    for (int r = 0; r < 16; ++r) wqv[r] = __builtin_amdgcn_raw_buffer_load_b32(rq, rg * (16 * 8192) + cp * 4, r * 8192, AUX_W);
    const __amdgpu_buffer_rsrc_t r1 = __builtin_amdgcn_make_buffer_rsrc((void*)(p.w1 + f0), 0, hwg ? 511 * FF * 2 + 32 : 0, RSRC_W3);
#pragma unroll
    for (int r = 0; r < 16; ++r) w1v[r] = __builtin_amdgcn_raw_buffer_load_b32(r1, rg * (16 * FF * 2) + cp * 4, r * FF * 2, AUX_W);
    SCHED();
  }
  if (wC < 4) {
    const unsigned b1v = p.b1[f0 + ocol];
    SCHED();
    float* xs = (float*)(lds + L_XS);
    float* ys = (float*)(lds + L_YS);
    float* red = (float*)(lds + L_RED);
    float* redh = (float*)(lds + L_REDH);
    float xv[8], xn[8];
#pragma unroll
    for (int i = 0; i < 4; ++i) { xv[2 * i] = bflo(xr[i]); xv[2 * i + 1] = bfhi(xr[i]); }
    ln_row(xv, xn);
    *(f32x4*)(xs + w * DM + 8 * lane) = f32x4{xn[0], xn[1], xn[2], xn[3]};
    *(f32x4*)(xs + w * DM + 8 * lane + 4) = f32x4{xn[4], xn[5], xn[6], xn[7]};
    const u64 e_ = ((u64)__builtin_amdgcn_readfirstlane((unsigned)(e_raw >> 32)) << 32) | (u64)__builtin_amdgcn_readfirstlane((unsigned)e_raw);
    const u64 tag40 = ((e_ >> 24) * 0x5DEECE66DULL + 0xBULL) & ((1ULL << 40) - 1);
    Tag tag; tag.lo8 = (unsigned)(tag40 & 0xff) << 24; tag.hi = (unsigned)(tag40 >> 8);
    lds_set(lb + L_FA + 4 * w, ltag);
    STAMP(1);
    lds_wait4(lb + L_FA, ltag);
    red[w * 64 + lane] = gemv16(wqv, xs, rg, lane);
    lds_set(lb + L_FB + 4 * w, ltag);
    const __amdgpu_buffer_rsrc_t rqx = __builtin_amdgcn_make_buffer_rsrc((void*)(ws + G_QX + kbt * 4096 + kg * 512), 0, 4096, RSRC_W3);
    v4u gq[4];
    if (w == 0) {
      lds_wait4(lb + L_FB, ltag);
      const float q = (red[lane] + red[64 + lane]) + (red[128 + lane] + red[192 + lane]);
      gstore(ws + G_QX + ob * 4096 + g * 512 + 16 * j + ocol, f2bf(q), tag);
      STAMP(2);
    }
    if (hwg) {
      float y[8];
      ln_row(xn, y);
      *(f32x4*)(ys + w * DM + 8 * lane) = f32x4{y[0], y[1], y[2], y[3]};
      *(f32x4*)(ys + w * DM + 8 * lane + 4) = f32x4{y[4], y[5], y[6], y[7]};
      lds_set(lb + L_FC + 4 * w, ltag);
      lds_wait4(lb + L_FC, ltag);
      redh[w * 64 + lane] = gemv16(w1v, ys, rg, lane);
      lds_set(lb + L_FD + 4 * w, ltag);
      if (w == 1) {
        lds_wait4(lb + L_FD, ltag);
        const float hp = (redh[lane] + redh[64 + lane]) + (redh[128 + lane] + redh[192 + lane]);
        const float pre = rbf(rbf(hp) + bflo(b1v));
        const float hv = 0.5f * pre * (1.0f + tanhf(0.7978845608028654f * (pre + 0.044715f * pre * pre * pre)));
        gstore(ws + G_HX + ob * 2048 + f0 + ocol, f2bf(hv), tag);
      }
    }
    if (w == 0) {
      poll_pairs<4>(rqx, lane * 16, 1024, 0u, tag, gq);
#pragma unroll
      for (int k = 0; k < 4; ++k) ((unsigned*)(lds + L_QS))[64 * k + lane] = (gq[k].x & 0xffffu) | (gq[k].z << 16);
      *(v2u*)(lds + L_TAG) = v2u{tag.lo8, tag.hi};
      lds_set(lb + L_FQ, ltag);
      STAMP(3);
    }
  }
  SCHED();
  ISSUE_WINDOW(true);
  lds_wait1(lb + L_FQ, ltag);
  Tag tag;
  { const v2u t = *(const v2u*)(lds + L_TAG); tag.lo8 = __builtin_amdgcn_readfirstlane(t.x); tag.hi = __builtin_amdgcn_readfirstlane(t.y); }
  STAMP(4);
  const v4u qf = *(const v4u*)(lds + L_QS + ((lane & 15) >> 2) * 256 + ((lane & 3) + 4 * (lane >> 4)) * 16);

  float m = -INFINITY, lsum = 0.f;
  f32x4 acc[8];
#pragma unroll
  for (int i = 0; i < 8; ++i) acc[i] = f32x4{0.f, 0.f, 0.f, 0.f};
  float sA[8], sB[8];
#pragma nounroll
  for (int t = 0; t < nmine; t += 2) {
    const bool na = t + 2 < nmine, nb = t + 3 < nmine;
    if (w >= 4 && t + 2 * LATESIDE >= nmine) lds_set(lb + L_FL + 4 * (w - 4), ltag);
    const size_t oa = (size_t)(na ? t + 2 : 0) * 32768, ob2 = (size_t)(nb ? t + 3 : 0) * 32768;
    qk_tile(A.k, qf, sA, kb + oa, na, koff, 0.f);
    pv_tile(A.v, sA, m, lsum, acc, lane, vb + oa, na, voff, A.k, kb + oa, koff);
    qk_tile(B.k, qf, sB, kb + ob2, nb, koff, t + 1 < nmine ? 0.f : -INFINITY);
    pv_tile(B.v, sB, m, lsum, acc, lane, vb + ob2, nb, voff, B.k, kb + ob2, koff);
  }
  lsum = sum_rows(lsum);
  STAMP(5);
  if (w >= 4) { stamp[1] = 0; stamp[2] = 0; stamp[3] = __builtin_amdgcn_s_memtime() - c0_; }
  float* om = (float*)(lds + L_OM);
  float* sm = (float*)(lds + L_SM);
  float* sl = (float*)(lds + L_SL);
  *(f32x4*)(om + w * 512 + lane * 8) = f32x4{acc[0][0], acc[1][0], acc[2][0], acc[3][0]};
  *(f32x4*)(om + w * 512 + lane * 8 + 4) = f32x4{acc[4][0], acc[5][0], acc[6][0], acc[7][0]};
  if (lane < 16 && (lane & 3) == 0) { sm[(lane >> 2) * 8 + w] = m; sl[(lane >> 2) * 8 + w] = lsum; }
  const int r8 = lane >> 3, c8 = lane & 7;

  v4u bw[2][8];
  if (w < 4) {
    lds_wait4(lb + L_FL, ltag);
    v4u t16[16];
    const __amdgpu_buffer_rsrc_t rwo = __builtin_amdgcn_make_buffer_rsrc((void*)(p.wo + (size_t)(g * 512 + 64 * s) * DM + 128 * w), 0, ((ABL_RED & 1) || ((ABL_RED & 8) && w != b)) ? 0 : 63 * 1024 + 256, RSRC_W3);
#pragma unroll
    for (int r = 0; r < 16; ++r) t16[r] = __builtin_amdgcn_raw_buffer_load_b128(rwo, (lane >> 4) * 1024 + (lane & 15) * 16, r * 4096, AUX_W2);
    v4u w2v[8];
    const int f0 = 256 * g + 64 * w;
    const __amdgpu_buffer_rsrc_t rw2 = __builtin_amdgcn_make_buffer_rsrc((void*)(p.w2 + (size_t)(g < 7 ? f0 : 0) * DM + 64 * s), 0, (g < 7 && !(ABL_RED & 2) && !((ABL_RED & 16) && w != b)) ? 63 * 1024 + 128 : 0, RSRC_W3);
#pragma unroll
    for (int i = 0; i < 8; ++i) w2v[i] = __builtin_amdgcn_raw_buffer_load_b128(rw2, r8 * 1024 + c8 * 16, i * 8192, AUX_W2);
    const u64* hsrc = ws + G_HX + b * 2048 + f0 + lane;
    u64 hg = gload(hsrc);
    SCHED();
#pragma unroll
    for (int ks = 0; ks < 2; ++ks)
#pragma unroll
      for (int jj = 0; jj < 4; ++jj)
#pragma unroll
        for (int k2 = 0; k2 < 4; ++k2) {
          bw[ks][2 * jj][k2] = __builtin_amdgcn_perm(t16[8 * ks + 2 * k2 + 1][jj], t16[8 * ks + 2 * k2][jj], 0x05040100u);
          bw[ks][2 * jj + 1][k2] = __builtin_amdgcn_perm(t16[8 * ks + 2 * k2 + 1][jj], t16[8 * ks + 2 * k2][jj], 0x07060302u);
        }
    if (g < 7 && !(ABL_RED & 4)) {
      if (!__all(gok(hg, tag))) {
        unsigned spin = 0;
        do { __builtin_amdgcn_s_sleep(PSLEEP); hg = gload(hsrc); } while (!__all(gok(hg, tag)) && ++spin < SPIN_CAP);
      }
      const float hl = bflo((unsigned)hg);
      float ma[8];
#pragma unroll
      for (int e = 0; e < 8; ++e) ma[e] = 0.f;
#pragma unroll
      for (int i = 0; i < 8; ++i) {
        const float hv = __shfl(hl, 8 * i + r8);
#pragma unroll
        for (int e = 0; e < 4; ++e) { ma[2 * e] = fmaf(hv, bflo(w2v[i][e]), ma[2 * e]); ma[2 * e + 1] = fmaf(hv, bfhi(w2v[i][e]), ma[2 * e + 1]); }
      }
      const float r = treduce8(ma, lane);
      gstore(ws + G_MX + ((size_t)(b * 64 + 8 * s + c8) * 32 + 4 * g + w) * 8 + r8, enc24(r), tag);
    }
  }
  STAMP(6);
  const unsigned xp = *(const GAS unsigned*)(p.x + b * DM + cb * 8 + 2 * (lane & 3));
  WG_BARRIER();
  {
    const int h = tid >> 7;
    const f32x4 m0 = *(const f32x4*)(sm + h * 8), m1 = *(const f32x4*)(sm + h * 8 + 4);
    float ov[8];
#pragma unroll
    for (int ww = 0; ww < 8; ++ww) ov[ww] = om[ww * 512 + tid];
    const f32x4 l0 = *(const f32x4*)(sl + h * 8), l1 = *(const f32x4*)(sl + h * 8 + 4);
    SCHED();
    const float M = fmaxf(fmaxf(fmaxf(m0[0], m0[1]), fmaxf(m0[2], m0[3])), fmaxf(fmaxf(m1[0], m1[1]), fmaxf(m1[2], m1[3])));
    float O = 0.f, L = 0.f;
#pragma unroll
    for (int ww = 0; ww < 4; ++ww) { const float f = __builtin_amdgcn_exp2f(m0[ww] - M); O = fmaf(f, ov[ww], O); L = fmaf(f, l0[ww], L); }
#pragma unroll
    for (int ww = 0; ww < 4; ++ww) { const float f = __builtin_amdgcn_exp2f(m1[ww] - M); O = fmaf(f, ov[4 + ww], O); L = fmaf(f, l1[ww], L); }
    gstore(ws + G_PX + (size_t)(kpair * 8 + ks) * 512 + tid, enc24(O), tag);
    const int d = tid & 127;
    if (d < 4) {
      const unsigned u = (d & 2) ? asu(L) : asu(M);
      gstore(ws + G_PS + (size_t)((kpair * 4 + h) * 8 + ks) * 4 + d, (d & 1) ? (u & 0xffffu) : (u >> 16), tag);
    }
  }
  STAMP(7);
  if (ABL_T & 2) {} else if (w == 4 || (DUP1 && w == 6)) {
    if (DUP1 && w == 6) __builtin_amdgcn_s_sleep(DUP1);
    const __amdgpu_buffer_rsrc_t rp = __builtin_amdgcn_make_buffer_rsrc((void*)(ws + G_PX + (size_t)pair * 4096 + 64 * s), 0, 8 * 4096, RSRC_W3);
    const __amdgpu_buffer_rsrc_t rs = __builtin_amdgcn_make_buffer_rsrc((void*)(ws + G_PS + (size_t)(pair * 4 + (s >> 1)) * 32), 0, 256, RSRC_W3);
    const int poff = (lane >> 5) * 4096 + (lane & 31) * 16;
    v4u gp[4]; v2u gs;
#pragma nounroll
    for (unsigned spin = 0; spin < SPIN_CAP; ++spin) {
      asm volatile("" ::: "memory");
      gs = __builtin_amdgcn_raw_buffer_load_b64(rs, (lane & 31) * 8, 0, AUX_SC1);
#pragma unroll
      for (int k = 0; k < 4; ++k) gp[k] = __builtin_amdgcn_raw_buffer_load_b128(rp, poff, k * 8192, AUX_SC1);
      const unsigned bad = gbad2(gp[0], tag) | gbad2(gp[1], tag) | gbad2(gp[2], tag) | gbad2(gp[3], tag) | (gs.y ^ tag.hi) | ((gs.x ^ tag.lo8) & 0xff000000u);
      if (__all(bad == 0u)) break;
      __builtin_amdgcn_s_sleep(PSLEEP);
    }
    STAMP(8);
    const unsigned sv = gs.x & 0xffffu;
    const float Mk = asf((asu(dpp<0x00>(asf(sv))) << 16) | asu(dpp<0x55>(asf(sv))));
    const float Lk = asf((asu(dpp<0xAA>(asf(sv))) << 16) | asu(dpp<0xFF>(asf(sv))));
    float M = fmaxf(Mk, dpp<ROR4>(Mk)); M = fmaxf(M, dpp<ROR8>(M));
    { auto t = __builtin_amdgcn_permlane16_swap(asu(M), asu(M), false, false); M = fmaxf(asf(t[0]), asf(t[1])); }
    const float fk = __builtin_amdgcn_exp2f(Mk - M);
    float L = fk * Lk; L += dpp<ROR4>(L); L += dpp<ROR8>(L); L = tr16(L, L);
    float o0 = 0.f, o1 = 0.f;
#pragma unroll
    for (int k = 0; k < 4; ++k) {
      const float fe = asf(__builtin_amdgcn_readlane(asu(fk), 8 * k)), fo = asf(__builtin_amdgcn_readlane(asu(fk), 8 * k + 4));
      const float f = (lane >> 5) ? fo : fe;
      o0 = fmaf(f, asf(gp[k].x << 8), o0); o1 = fmaf(f, asf(gp[k].z << 8), o1);
    }
    const float ot = tr32(o0, o1);
    const int e = 2 * (lane & 31) + (lane >> 5);
    ((u16*)(lds + L_OV))[(e & 32) + (e & 3) * 8 + ((e >> 2) & 7)] = (u16)f2bf(RCP ? ot * __builtin_amdgcn_rcpf(L) : ot / L);
    lds_set(lb + L_FO, ltag);
  } else if (w < 4) {
    const __amdgpu_buffer_rsrc_t rsx = __builtin_amdgcn_make_buffer_rsrc((void*)(ws + G_SX + ((size_t)(b * 64 + 16 * w) * 64 + cb) * 8), 0, 16 * 4096, RSRC_W3);
    lds_wait1(lb + L_FO, ltag);
    const v4u a0 = *(const v4u*)(lds + L_OV + (lane >> 4) * 16), a1 = *(const v4u*)(lds + L_OV + 64 + (lane >> 4) * 16);
    f32x4 d[8];
#pragma unroll
    for (int jx = 0; jx < 8; ++jx) d[jx] = mfma(a0, bw[0][jx], f32x4{0.f, 0.f, 0.f, 0.f});
#pragma unroll
    for (int jx = 0; jx < 8; ++jx) d[jx] = mfma(a1, bw[1][jx], d[jx]);
    const int g4 = lane >> 4;
    const float v0 = (g4 & 2) ? ((g4 & 1) ? d[6][0] : d[4][0]) : ((g4 & 1) ? d[2][0] : d[0][0]);
    const float v1 = (g4 & 2) ? ((g4 & 1) ? d[7][0] : d[5][0]) : ((g4 & 1) ? d[3][0] : d[1][0]);
    __builtin_amdgcn_raw_buffer_store_b128(v4u{enc24(v0) | tag.lo8, tag.hi, enc24(v1) | tag.lo8, tag.hi}, rsx, (lane & 15) * 4096 + g4 * 16, 0, AUX_SC1);
    STAMP(9);
  } else if (w == 5 && !(ABL_T & 1)) {
    const unsigned b2p = *(const GAS unsigned*)(p.b2 + cb * 8 + 2 * (lane & 3));
    float m0, m1;
    gather8<2>(ws + G_MX + (size_t)(b * 64 + cb) * 256, 28, tag, lane, m0, m1);
    m0 = rbf(rbf(m0) + bflo(b2p)); m1 = rbf(rbf(m1) + bfhi(b2p));
    float2 gt;
    gt.x = rbf(m0 / (1.0f + __expf(-m0))); gt.y = rbf(m1 / (1.0f + __expf(-m1)));
    float s0, s1;
    gather8<4>(ws + G_SX + (size_t)(b * 64 + cb) * 512, 64, tag, lane, s0, s1);
    STAMP(10);
    const float o0 = bflo(xp) + rbf(rbf(s0) * gt.x), o1 = bfhi(xp) + rbf(rbf(s1) * gt.y);
    if (lane < 4) *(GAS unsigned*)(p.out + b * DM + cb * 8 + 2 * lane) = cvtpk(o0, o1);
    if (bid == 0 && lane == 0) __hip_atomic_store((GAS u64*)(ws + G_EPOCH), ((u64)tag.hi << 32) | tag.lo8, RLX_AGENT);
  }
  STAMP(11);
  if (lane == 0) {
#pragma unroll
    for (int k = 0; k < NSTAMP; ++k) ws[STAMP_OFF / 8 + (bid * 8 + w) * NSTAMP + k] = stamp[k];
  }
}

__global__ void k_report(const u64* st) {
  if (threadIdx.x != 0) return;
  u64 t0 = ~0ull;
  for (int i = 0; i < 2048; ++i) if (st[i * NSTAMP] < t0) t0 = st[i * NSTAMP];
  for (int kk = 0; kk < NSTAMP; ++kk) {
    u64 mn = ~0ull, mx = 0, sum = 0; int n = 0;
    for (int i = 0; i < 2048; ++i) { if (kk == 3 && (i & 7) >= 4) continue; u64 t = st[i * NSTAMP + kk]; if (!t) continue; t -= t0; if (t < mn) mn = t; if (t > mx) mx = t; sum += t; ++n; }
    if (n) printf("s%d min %d avg %d max %d (x10ns) n %d\n", kk, (int)mn, (int)(sum / n), (int)mx, n);
  }
  for (int i = 4; i < 2048; i += 8 * 37) printf("wave %d: cycles %d over %d x10ns -> %d MHz\n", i, (int)st[i * NSTAMP + 3], (int)(st[i * NSTAMP + 5] - st[i * NSTAMP]), (int)(st[i * NSTAMP + 3] * 100 / (st[i * NSTAMP + 5] - st[i * NSTAMP])));
}
}

extern "C" void kernel_launch(void* const* d_in, const int* in_sizes, int n_in,
                              void* d_out, int out_size, void* d_ws, size_t ws_size,
                              hipStream_t stream) {
  Params p;
  p.x = (const u16*)d_in[0]; p.k = (const u16*)d_in[1]; p.v = (const u16*)d_in[2];
  p.wq = (const u16*)d_in[3]; p.wo = (const u16*)d_in[4];
  p.w1 = (const u16*)d_in[6]; p.b1 = (const u16*)d_in[7]; p.w2 = (const u16*)d_in[8]; p.b2 = (const u16*)d_in[9];
  p.out = (u16*)d_out; p.ws = (u64*)d_ws;
  hipLaunchKernelGGL(fused_decode, dim3(NWG), dim3(NTHR), 0, stream, p);
  hipLaunchKernelGGL(k_report, dim3(1), dim3(64), 0, stream, (const u64*)((char*)d_ws + STAMP_OFF));
}
```

```cpp
#include <hip/hip_runtime.h>
#include <hip/hip_bf16.h>
#include <stdint.h>

namespace {

typedef unsigned v4u __attribute__((ext_vector_type(4)));
typedef unsigned v2u __attribute__((ext_vector_type(2)));
typedef float f32x4 __attribute__((ext_vector_type(4)));
typedef short bf16x8 __attribute__((ext_vector_type(8)));
typedef unsigned long long u64;
typedef unsigned short u16;
#define GAS __attribute__((address_space(1)))
#define RLX_AGENT __ATOMIC_RELAXED, __HIP_MEMORY_SCOPE_AGENT

constexpr int DM = 512, SK = 16384, FF = 1792;
constexpr int NWG = 256, NTHR = 512;
constexpr int W = 32;
constexpr int RSRC_W3 = 0x00020000;
constexpr int AUX_NT = 2, AUX_SC1 = 16;
#define AUX_W 0
#define AUX_K 2
#define AUX_V 2
#define TOPWIN 0
#define PW 1
#define AUX_W2 0
#define LATEK 0
#define ABL_T 0
#define XMAP 2
#define SKT 0
#define PRET 0
#define LATESIDE 3
#define HSTR 4096
#define DUP1 0
#define RCP 0
#define ABL_RED 0
#define DRY 0
#define AUX_G 16
#define PSLEEP 2
constexpr unsigned SPIN_CAP = 1u << 16;

constexpr size_t G_EPOCH = 0;
constexpr size_t G_QX = 512;
constexpr size_t G_PS = G_QX + 4 * 4096;
constexpr size_t G_PX = G_PS + 32 * 8 * 16;
constexpr size_t G_SX = G_PX + 32 * 8 * 512;
constexpr size_t G_HX = G_SX + 4 * 64 * 64 * 8;
constexpr size_t G_MX = G_HX + 4 * 2048;
constexpr size_t G_END = G_MX + 4 * 64 * 32 * 8;
constexpr size_t G_SHADOW = 524288;
static_assert(G_END <= G_SHADOW, "ws");

constexpr int WO_LD = 1040;
constexpr int L_OM = 0;
constexpr int L_XS = 16384, L_YS = L_XS + 8192;
constexpr int L_RED = 32768, L_REDH = L_RED + 1024, L_QS = L_REDH + 1024, L_SM = L_QS + 1024, L_SL = L_SM + 128,
              L_GATE = L_SL + 128, L_FA = L_GATE + 64, L_FB = L_FA + 16, L_FC = L_FB + 16, L_FD = L_FC + 16,
              L_FQ = L_FD + 16, L_TAG = L_FQ + 16, L_FO = L_TAG + 16, L_FL = L_FO + 16, L_OV = L_FA + 128, L_WO = L_OV + 8 * 256, L_END = L_WO + 64 * WO_LD;
constexpr int LDS_BYTES = 108 * 1024;
static_assert(L_END <= LDS_BYTES && L_WO % 16 == 0 && L_OV % 16 == 0 && L_FA % 16 == 0, "lds");

struct Params {
  const u16* x; const u16* k; const u16* v; const u16* wq; const u16* wo;
  const u16* w1; const u16* b1; const u16* w2; const u16* b2;
  u16* out; u64* ws;
};

__device__ __forceinline__ float asf(unsigned u) { return __builtin_bit_cast(float, u); }
__device__ __forceinline__ unsigned asu(float f) { return __builtin_bit_cast(unsigned, f); }
__device__ __forceinline__ float bflo(unsigned u) { return asf(u << 16); }
__device__ __forceinline__ float bfhi(unsigned u) { return asf(u & 0xffff0000u); }
__device__ __forceinline__ unsigned f2bf(float f) { return (unsigned)__builtin_bit_cast(u16, (__bf16)f); }
__device__ __forceinline__ float rbf(float f) { return (float)(__bf16)f; }
__device__ __forceinline__ unsigned cvtpk(float lo, float hi) {
  typedef __bf16 bf2 __attribute__((ext_vector_type(2)));
  bf2 r; r.x = (__bf16)lo; r.y = (__bf16)hi;
  return __builtin_bit_cast(unsigned, r);
}
template <int CTRL> __device__ __forceinline__ float dpp(float x) {
  return __builtin_bit_cast(float, __builtin_amdgcn_mov_dpp(__builtin_bit_cast(int, x), CTRL, 0xf, 0xf, true));
}
constexpr int XOR1 = 0xB1, XOR2 = 0x4E, HMIR = 0x141, MIR = 0x140, ROR8 = 0x128, ROR4 = 0x124;
__device__ __forceinline__ float tr32(float a, float b) {
  auto r = __builtin_amdgcn_permlane32_swap(asu(a), asu(b), false, false);
  return asf(r[0]) + asf(r[1]);
}
__device__ __forceinline__ float tr16(float a, float b) {
  auto r = __builtin_amdgcn_permlane16_swap(asu(a), asu(b), false, false);
  return asf(r[0]) + asf(r[1]);
}
__device__ __forceinline__ float max_rows(float x) {
  auto s = __builtin_amdgcn_permlane16_swap(asu(x), asu(x), false, false);
  x = fmaxf(asf(s[0]), asf(s[1]));
  auto t = __builtin_amdgcn_permlane32_swap(asu(x), asu(x), false, false);
  return fmaxf(asf(t[0]), asf(t[1]));
}
__device__ __forceinline__ float sum_rows(float x) { x = tr16(x, x); return tr32(x, x); }
__device__ __forceinline__ float wave_sum(float x) {
  x += dpp<XOR1>(x); x += dpp<XOR2>(x); x += dpp<HMIR>(x); x += dpp<MIR>(x);
  return sum_rows(x);
}
__device__ __forceinline__ float treduce8(const float (&v)[8], int lane) {
  const float u0 = tr32(v[0], v[4]), u1 = tr32(v[1], v[5]), u2 = tr32(v[2], v[6]), u3 = tr32(v[3], v[7]);
  const float w0 = tr16(u0, u2), w1 = tr16(u1, u3);
  const float t0 = w0 + dpp<ROR8>(w0), t1 = w1 + dpp<ROR8>(w1);
  return (lane & 8) ? t1 : t0;
}
__device__ __forceinline__ void ln_row(const float (&v)[8], float (&o)[8]) {
  float s = 0.f;
#pragma unroll
  for (int i = 0; i < 8; ++i) s += v[i];
  s = wave_sum(s);
  const float mean = s * (1.0f / DM);
  float ss = 0.f;
#pragma unroll
  for (int i = 0; i < 8; ++i) { const float d = v[i] - mean; ss = fmaf(d, d, ss); }
  ss = wave_sum(ss);
  const float var_b = rbf(ss * (1.0f / DM)), m_b = rbf(mean);
  const float den = rbf(sqrtf(rbf(var_b + 1e-5f)));
#pragma unroll
  for (int i = 0; i < 8; ++i) o[i] = rbf(rbf(v[i] - m_b) / den);
}
#define WG_BARRIER() asm volatile("s_waitcnt lgkmcnt(0)\n\ts_barrier" ::: "memory")
#define SCHED() __builtin_amdgcn_sched_barrier(0)
#define STAMP(k) do {} while (0)

struct Tag { unsigned lo8, hi; };
__device__ __forceinline__ void gstore(u64* p, unsigned payload24, Tag t) {
  __hip_atomic_store((GAS u64*)p, ((u64)t.hi << 32) | (u64)(payload24 | t.lo8), RLX_AGENT);
}
__device__ __forceinline__ u64 gload(const u64* p) { return __hip_atomic_load((const GAS u64*)p, RLX_AGENT); }
__device__ __forceinline__ bool gok(u64 g, Tag t) {
  return (unsigned)(g >> 32) == t.hi && ((unsigned)g & 0xff000000u) == t.lo8;
}
__device__ __forceinline__ unsigned gbad2(v4u g, Tag t) {
  return (((g.x ^ t.lo8) | (g.z ^ t.lo8)) & 0xff000000u) | (g.y ^ t.hi) | (g.w ^ t.hi);
}
__device__ __forceinline__ bool gok2(v4u g, Tag t) { return gbad2(g, t) == 0u; }
__device__ __forceinline__ unsigned enc24(float f) { unsigned u = asu(f); u += 0x7fu + ((u >> 8) & 1u); return u >> 8; }
__device__ __forceinline__ float dec24(u64 g) { return asf((unsigned)g << 8); }

__device__ __forceinline__ f32x4 mfma(v4u a, v4u b, f32x4 c) {
  return __builtin_amdgcn_mfma_f32_16x16x32_bf16(__builtin_bit_cast(bf16x8, a), __builtin_bit_cast(bf16x8, b), c, 0, 0, 0);
}

__device__ __forceinline__ float gemv16(const unsigned (&wv)[16], const float* vec, int rg, int lane) {
  float acc[8];
#pragma unroll
  for (int bb = 0; bb < 4; ++bb) {
    float a0 = 0.f, a1 = 0.f;
#pragma unroll
    for (int q4 = 0; q4 < 4; ++q4) {
      const f32x4 x0 = *(const f32x4*)(vec + bb * DM + 16 * rg + 4 * q4);
#pragma unroll
      for (int r = 0; r < 4; ++r) { a0 = fmaf(x0[r], bflo(wv[4 * q4 + r]), a0); a1 = fmaf(x0[r], bfhi(wv[4 * q4 + r]), a1); }
    }
    acc[bb * 2] = a0; acc[bb * 2 + 1] = a1;
  }
  return treduce8(acc, lane);
}
__device__ __forceinline__ void lds_set(unsigned addr, unsigned v) {
  asm volatile("s_waitcnt lgkmcnt(0)\n\tds_write_b32 %0, %1" :: "v"(addr), "v"(v) : "memory");
}
__device__ __forceinline__ void lds_wait4(unsigned addr, unsigned v) {
#pragma nounroll
  for (unsigned spin = 0; spin < (1u << 22); ++spin) {
    v4u f;
    asm volatile("ds_read_b128 %0, %1\n\ts_waitcnt lgkmcnt(0)" : "=v"(f) : "v"(addr) : "memory");
    if (__builtin_amdgcn_readfirstlane((f.x == v) & (f.y == v) & (f.z == v) & (f.w == v))) break;
    __builtin_amdgcn_s_sleep(1);
  }
}
__device__ __forceinline__ void lds_wait1(unsigned addr, unsigned v) {
#pragma nounroll
  for (unsigned spin = 0; spin < (1u << 22); ++spin) {
    unsigned f;
    asm volatile("ds_read_b32 %0, %1\n\ts_waitcnt lgkmcnt(0)" : "=v"(f) : "v"(addr) : "memory");
    if (__builtin_amdgcn_readfirstlane(f) == v) break;
    __builtin_amdgcn_s_sleep(1);
  }
}

struct Tile { v4u k[8]; v4u v[8]; };
__device__ __forceinline__ void ld8(v4u (&d)[8], const u16* base, bool valid, int off) {
  const __amdgpu_buffer_rsrc_t r = __builtin_amdgcn_make_buffer_rsrc((void*)base, 0, valid ? HSTR + 4096 : 0, RSRC_W3);
#pragma unroll
  for (int i = 0; i < 8; ++i) d[i] = __builtin_amdgcn_raw_buffer_load_b128(r, off + (i & 3) * 1024, (i >> 2) * HSTR, AUX_NT);
  SCHED();
}
__device__ __forceinline__ void qk_tile(v4u (&k)[8], v4u qf, float (&sc)[8], const u16* kn, bool nvalid, int koff, float bias) {
  constexpr float C2 = 0.08838834764831845f * 1.4426950408889634f;
  f32x4 d[8];
#pragma unroll
  for (int i = 0; i < 8; ++i) d[i] = mfma(k[i], qf, f32x4{0.f, 0.f, 0.f, 0.f});
  SCHED();
  ld8(k, kn, nvalid, koff);
#pragma unroll
  for (int i = 0; i < 8; ++i) sc[i] = fmaf((dpp<0x00>(d[i][0]) + dpp<0x55>(d[i][1])) + (dpp<0xAA>(d[i][2]) + dpp<0xFF>(d[i][3])), C2, bias);
}
__device__ __forceinline__ void pv_tile(v4u (&v)[8], const float (&sc)[8], float& m, float& l, f32x4 (&acc)[8], int lane,
                                        const u16* vn, bool nvalid, int voff, v4u (&k)[8], const u16* kn, int koff) {
  float tm = fmaxf(fmaxf(fmaxf(sc[0], sc[1]), fmaxf(sc[2], sc[3])), fmaxf(fmaxf(sc[4], sc[5]), fmaxf(sc[6], sc[7])));
  tm = max_rows(tm);
  if (__any(tm > m)) {
    const float mn = fmaxf(m, tm), al = __builtin_amdgcn_exp2f(m - mn);
    m = mn; l *= al;
    const float ah = __shfl(al, (lane >> 4) * 4);
#pragma unroll
    for (int j = 0; j < 8; ++j) acc[j] *= ah;
  }
  v4u pk;
#pragma unroll
  for (int k = 0; k < 4; ++k) {
    const float p0 = __builtin_amdgcn_exp2f(sc[2 * k] - m), p1 = __builtin_amdgcn_exp2f(sc[2 * k + 1] - m);
    l += p0 + p1; pk[k] = cvtpk(p0, p1);
  }
#pragma unroll
  for (int jj = 0; jj < 4; ++jj) {
    v4u lo, hi;
#pragma unroll
    for (int k2 = 0; k2 < 4; ++k2) {
      lo[k2] = __builtin_amdgcn_perm(v[2 * k2 + 1][jj], v[2 * k2][jj], 0x05040100u);
      hi[k2] = __builtin_amdgcn_perm(v[2 * k2 + 1][jj], v[2 * k2][jj], 0x07060302u);
    }
    acc[2 * jj] = mfma(pk, lo, acc[2 * jj]); acc[2 * jj + 1] = mfma(pk, hi, acc[2 * jj + 1]);
  }
  SCHED();
  ld8(v, vn, nvalid, voff);
}
template <int NK> __device__ __forceinline__ void poll_pairs(__amdgpu_buffer_rsrc_t r, int voff, int kstride, unsigned skip, Tag tag, v4u (&gv)[NK]) {
  int off[NK];
#pragma unroll
  for (int k = 0; k < NK; ++k) { off[k] = ((skip >> k) & 1u) ? 0x40000000 : voff; gv[k] = v4u{0u, 0u, 0u, 0u}; }
#pragma nounroll
  for (unsigned spin = 0; spin < SPIN_CAP; ++spin) {
    asm volatile("" ::: "memory");
    v4u t[NK];
#pragma unroll
    for (int k = 0; k < NK; ++k) t[k] = __builtin_amdgcn_raw_buffer_load_b128(r, off[k], k * kstride, AUX_SC1);
    bool pend = false;
#pragma unroll
    for (int k = 0; k < NK; ++k) {
      const bool got = gbad2(t[k], tag) == 0u;
      const bool live = off[k] == voff;
      if (got & live) { gv[k].x = t[k].x; gv[k].z = t[k].z; off[k] = 0x40000000; }
      pend |= live & !got;
    }
    if (!__any(pend)) break;
    __builtin_amdgcn_s_sleep(PSLEEP);
  }
}
template <int NK> __device__ __forceinline__ void gather8(const u64* src, int nparts, Tag tag, int lane, float& s0, float& s1) {
  const __amdgpu_buffer_rsrc_t r = __builtin_amdgcn_make_buffer_rsrc((void*)src, 0, NK * 1024, RSRC_W3);
  v4u gv[NK];
  unsigned skip = 0u;
#pragma unroll
  for (int k = 0; k < NK; ++k) skip |= (16 * k + (lane >> 2) >= nparts) ? (1u << k) : 0u;
  poll_pairs<NK>(r, lane * 16, 1024, skip, tag, gv);
  s0 = 0.f; s1 = 0.f;
#pragma unroll
  for (int k = 0; k < NK; ++k) { s0 += asf(gv[k].x << 8); s1 += asf(gv[k].z << 8); }
  s0 += dpp<ROR4>(s0); s1 += dpp<ROR4>(s1);
  s0 += dpp<ROR8>(s0); s1 += dpp<ROR8>(s1);
  s0 = sum_rows(s0); s1 = sum_rows(s1);
}

__global__ void __launch_bounds__(NTHR, 2) fused_decode(Params p) {
  __shared__ __attribute__((aligned(16))) unsigned char lds[LDS_BYTES];
  const int tid = threadIdx.x, lane = tid & 63, w = __builtin_amdgcn_readfirstlane(tid >> 6);
  const int bid = blockIdx.x, g = bid & 7, j = bid >> 3, b = j >> 3, s = j & 7, pair = b * 8 + g, cb = g * 8 + s;
  const int kbt = g >> 1, kg = j >> 2, ks = (j & 3) * 2 + (g & 1);
  const int tile0 = (j & 3) * 128 + ((g & 1) ? 64 - SKT : 0), ntl = (g & 1) ? 64 + SKT : 64 - SKT;
  const int kpair = kbt * 8 + kg, nmine = (ntl - w + 7) >> 3;
  u64* ws = p.ws;
  const unsigned lb = (unsigned)(size_t)(__attribute__((address_space(3))) unsigned char*)lds;
  STAMP(0);

  if (tid < 32) ((unsigned*)(lds + L_FA))[tid] = 0u;
  const size_t kvoff = ((size_t)kpair * SK + tile0 * 32 + w * (HSTR == 4096 ? 32 : 16)) * 128;
  const u16* kb = p.k + kvoff;
  const u16* vb = p.v + kvoff;
  const int koff = ((lane & 15) >> 2) * 256 + (lane & 3) * 16 + (lane >> 4) * 64, voff = lane * 16;
  constexpr unsigned ltag = 1u;
  Tile A, B;
#define ISSUE_WINDOW(on) do { ld8(A.k, kb, on, koff); ld8(A.v, vb, on, voff); ld8(B.k, kb + 32768, on, koff); ld8(B.v, vb + 32768, on, voff); } while (0)
  const int cp = tid & 7, rg = tid >> 3;
  const bool hwg = j >= 18;
  const int f0 = 224 * g + 16 * (hwg ? j - 18 : 0);
  const int ob = lane >> 4, ocol = 2 * (lane & 7) + ((lane >> 3) & 1);
  u64 e_raw; v4u xr; unsigned wqv[16], w1v[16];
  int wA = w, wB = w, wC = w;
  asm volatile("" : "+s"(wA)); asm volatile("" : "+s"(wB)); asm volatile("" : "+s"(wC));
  WG_BARRIER();
  if (wA < 4) {
    e_raw = gload(ws + G_EPOCH);
    xr = *(const GAS v4u*)(p.x + w * DM + 8 * lane);
    const __amdgpu_buffer_rsrc_t rq = __builtin_amdgcn_make_buffer_rsrc((void*)(p.wq + g * 512 + 16 * j), 0, 512 * 8192 - 8192 + 32, RSRC_W3);
#pragma unroll
    for (int r = 0; r < 16; ++r) wqv[r] = __builtin_amdgcn_raw_buffer_load_b32(rq, rg * (16 * 8192) + cp * 4, r * 8192, AUX_W);
    const __amdgpu_buffer_rsrc_t r1 = __builtin_amdgcn_make_buffer_rsrc((void*)(p.w1 + f0), 0, hwg ? 511 * FF * 2 + 32 : 0, RSRC_W3);
#pragma unroll
    for (int r = 0; r < 16; ++r) w1v[r] = __builtin_amdgcn_raw_buffer_load_b32(r1, rg * (16 * FF * 2) + cp * 4, r * FF * 2, AUX_W);
    SCHED();
  }
  if (wC < 4) {
    const unsigned b1v = p.b1[f0 + ocol];
    SCHED();
    float* xs = (float*)(lds + L_XS);
    float* ys = (float*)(lds + L_YS);
    float* red = (float*)(lds + L_RED);
    float* redh = (float*)(lds + L_REDH);
    float xv[8], xn[8];
#pragma unroll
    for (int i = 0; i < 4; ++i) { xv[2 * i] = bflo(xr[i]); xv[2 * i + 1] = bfhi(xr[i]); }
    ln_row(xv, xn);
    *(f32x4*)(xs + w * DM + 8 * lane) = f32x4{xn[0], xn[1], xn[2], xn[3]};
    *(f32x4*)(xs + w * DM + 8 * lane + 4) = f32x4{xn[4], xn[5], xn[6], xn[7]};
    const u64 e_ = ((u64)__builtin_amdgcn_readfirstlane((unsigned)(e_raw >> 32)) << 32) | (u64)__builtin_amdgcn_readfirstlane((unsigned)e_raw);
    const u64 tag40 = ((e_ >> 24) * 0x5DEECE66DULL + 0xBULL) & ((1ULL << 40) - 1);
    Tag tag; tag.lo8 = (unsigned)(tag40 & 0xff) << 24; tag.hi = (unsigned)(tag40 >> 8);
    lds_set(lb + L_FA + 4 * w, ltag);
    STAMP(1);
    lds_wait4(lb + L_FA, ltag);
    red[w * 64 + lane] = gemv16(wqv, xs, rg, lane);
    lds_set(lb + L_FB + 4 * w, ltag);
    const __amdgpu_buffer_rsrc_t rqx = __builtin_amdgcn_make_buffer_rsrc((void*)(ws + G_QX + kbt * 4096 + kg * 512), 0, 4096, RSRC_W3);
    v4u gq[4];
    if (w == 0) {
      lds_wait4(lb + L_FB, ltag);
      const float q = (red[lane] + red[64 + lane]) + (red[128 + lane] + red[192 + lane]);
      gstore(ws + G_QX + ob * 4096 + g * 512 + 16 * j + ocol, f2bf(q), tag);
      STAMP(2);
    }
    if (hwg) {
      float y[8];
      ln_row(xn, y);
      *(f32x4*)(ys + w * DM + 8 * lane) = f32x4{y[0], y[1], y[2], y[3]};
      *(f32x4*)(ys + w * DM + 8 * lane + 4) = f32x4{y[4], y[5], y[6], y[7]};
      lds_set(lb + L_FC + 4 * w, ltag);
      lds_wait4(lb + L_FC, ltag);
      redh[w * 64 + lane] = gemv16(w1v, ys, rg, lane);
      lds_set(lb + L_FD + 4 * w, ltag);
      if (w == 1) {
        lds_wait4(lb + L_FD, ltag);
        const float hp = (redh[lane] + redh[64 + lane]) + (redh[128 + lane] + redh[192 + lane]);
        const float pre = rbf(rbf(hp) + bflo(b1v));
        const float hv = 0.5f * pre * (1.0f + tanhf(0.7978845608028654f * (pre + 0.044715f * pre * pre * pre)));
        gstore(ws + G_HX + ob * 2048 + f0 + ocol, f2bf(hv), tag);
      }
    }
    if (w == 0) {
      poll_pairs<4>(rqx, lane * 16, 1024, 0u, tag, gq);
#pragma unroll
      for (int k = 0; k < 4; ++k) ((unsigned*)(lds + L_QS))[64 * k + lane] = (gq[k].x & 0xffffu) | (gq[k].z << 16);
      *(v2u*)(lds + L_TAG) = v2u{tag.lo8, tag.hi};
      lds_set(lb + L_FQ, ltag);
      STAMP(3);
    }
  }
  SCHED();
  ISSUE_WINDOW(true);
  lds_wait1(lb + L_FQ, ltag);
  Tag tag;
  { const v2u t = *(const v2u*)(lds + L_TAG); tag.lo8 = __builtin_amdgcn_readfirstlane(t.x); tag.hi = __builtin_amdgcn_readfirstlane(t.y); }
  STAMP(4);
  const v4u qf = *(const v4u*)(lds + L_QS + ((lane & 15) >> 2) * 256 + ((lane & 3) + 4 * (lane >> 4)) * 16);

  float m = -INFINITY, lsum = 0.f;
  f32x4 acc[8];
#pragma unroll
  for (int i = 0; i < 8; ++i) acc[i] = f32x4{0.f, 0.f, 0.f, 0.f};
  float sA[8], sB[8];
#pragma nounroll
  for (int t = 0; t < nmine; t += 2) {
    const bool na = t + 2 < nmine, nb = t + 3 < nmine;
    if (w >= 4 && t + 2 * LATESIDE >= nmine) lds_set(lb + L_FL + 4 * (w - 4), ltag);
    const size_t oa = (size_t)(na ? t + 2 : 0) * 32768, ob2 = (size_t)(nb ? t + 3 : 0) * 32768;
    qk_tile(A.k, qf, sA, kb + oa, na, koff, 0.f);
    pv_tile(A.v, sA, m, lsum, acc, lane, vb + oa, na, voff, A.k, kb + oa, koff);
    qk_tile(B.k, qf, sB, kb + ob2, nb, koff, t + 1 < nmine ? 0.f : -INFINITY);
    pv_tile(B.v, sB, m, lsum, acc, lane, vb + ob2, nb, voff, B.k, kb + ob2, koff);
  }
  lsum = sum_rows(lsum);
  STAMP(5);
  float* om = (float*)(lds + L_OM);
  float* sm = (float*)(lds + L_SM);
  float* sl = (float*)(lds + L_SL);
  *(f32x4*)(om + w * 512 + lane * 8) = f32x4{acc[0][0], acc[1][0], acc[2][0], acc[3][0]};
  *(f32x4*)(om + w * 512 + lane * 8 + 4) = f32x4{acc[4][0], acc[5][0], acc[6][0], acc[7][0]};
  if (lane < 16 && (lane & 3) == 0) { sm[(lane >> 2) * 8 + w] = m; sl[(lane >> 2) * 8 + w] = lsum; }
  const int r8 = lane >> 3, c8 = lane & 7;

  v4u bw[2][8];
  if (w < 4) {
    lds_wait4(lb + L_FL, ltag);
    v4u t16[16];
    const __amdgpu_buffer_rsrc_t rwo = __builtin_amdgcn_make_buffer_rsrc((void*)(p.wo + (size_t)(g * 512 + 64 * s) * DM + 128 * w), 0, ((ABL_RED & 1) || ((ABL_RED & 8) && w != b)) ? 0 : 63 * 1024 + 256, RSRC_W3);
#pragma unroll
    for (int r = 0; r < 16; ++r) t16[r] = __builtin_amdgcn_raw_buffer_load_b128(rwo, (lane >> 4) * 1024 + (lane & 15) * 16, r * 4096, AUX_W2);
    v4u w2v[8];
    const int f0 = 256 * g + 64 * w;
    const __amdgpu_buffer_rsrc_t rw2 = __builtin_amdgcn_make_buffer_rsrc((void*)(p.w2 + (size_t)(g < 7 ? f0 : 0) * DM + 64 * s), 0, (g < 7 && !(ABL_RED & 2) && !((ABL_RED & 16) && w != b)) ? 63 * 1024 + 128 : 0, RSRC_W3);
#pragma unroll
    for (int i = 0; i < 8; ++i) w2v[i] = __builtin_amdgcn_raw_buffer_load_b128(rw2, r8 * 1024 + c8 * 16, i * 8192, AUX_W2);
    const u64* hsrc = ws + G_HX + b * 2048 + f0 + lane;
    u64 hg = gload(hsrc);
    SCHED();
#pragma unroll
    for (int ks = 0; ks < 2; ++ks)
#pragma unroll
      for (int jj = 0; jj < 4; ++jj)
#pragma unroll
        for (int k2 = 0; k2 < 4; ++k2) {
          bw[ks][2 * jj][k2] = __builtin_amdgcn_perm(t16[8 * ks + 2 * k2 + 1][jj], t16[8 * ks + 2 * k2][jj], 0x05040100u);
          bw[ks][2 * jj + 1][k2] = __builtin_amdgcn_perm(t16[8 * ks + 2 * k2 + 1][jj], t16[8 * ks + 2 * k2][jj], 0x07060302u);
        }
    if (g < 7 && !(ABL_RED & 4)) {
      if (!__all(gok(hg, tag))) {
        unsigned spin = 0;
        do { __builtin_amdgcn_s_sleep(PSLEEP); hg = gload(hsrc); } while (!__all(gok(hg, tag)) && ++spin < SPIN_CAP);
      }
      const float hl = bflo((unsigned)hg);
      float ma[8];
#pragma unroll
      for (int e = 0; e < 8; ++e) ma[e] = 0.f;
#pragma unroll
      for (int i = 0; i < 8; ++i) {
        const float hv = __shfl(hl, 8 * i + r8);
#pragma unroll
        for (int e = 0; e < 4; ++e) { ma[2 * e] = fmaf(hv, bflo(w2v[i][e]), ma[2 * e]); ma[2 * e + 1] = fmaf(hv, bfhi(w2v[i][e]), ma[2 * e + 1]); }
      }
      const float r = treduce8(ma, lane);
      gstore(ws + G_MX + ((size_t)(b * 64 + 8 * s + c8) * 32 + 4 * g + w) * 8 + r8, enc24(r), tag);
    }
  }
  STAMP(6);
  const unsigned xp = *(const GAS unsigned*)(p.x + b * DM + cb * 8 + 2 * (lane & 3));
  WG_BARRIER();
  {
    const int h = tid >> 7;
    const f32x4 m0 = *(const f32x4*)(sm + h * 8), m1 = *(const f32x4*)(sm + h * 8 + 4);
    float ov[8];
#pragma unroll
    for (int ww = 0; ww < 8; ++ww) ov[ww] = om[ww * 512 + tid];
    const f32x4 l0 = *(const f32x4*)(sl + h * 8), l1 = *(const f32x4*)(sl + h * 8 + 4);
    SCHED();
    const float M = fmaxf(fmaxf(fmaxf(m0[0], m0[1]), fmaxf(m0[2], m0[3])), fmaxf(fmaxf(m1[0], m1[1]), fmaxf(m1[2], m1[3])));
    float O = 0.f, L = 0.f;
#pragma unroll
    for (int ww = 0; ww < 4; ++ww) { const float f = __builtin_amdgcn_exp2f(m0[ww] - M); O = fmaf(f, ov[ww], O); L = fmaf(f, l0[ww], L); }
#pragma unroll
    for (int ww = 0; ww < 4; ++ww) { const float f = __builtin_amdgcn_exp2f(m1[ww] - M); O = fmaf(f, ov[4 + ww], O); L = fmaf(f, l1[ww], L); }
    gstore(ws + G_PX + (size_t)(kpair * 8 + ks) * 512 + tid, enc24(O), tag);
    const int d = tid & 127;
    if (d < 4) {
      const unsigned u = (d & 2) ? asu(L) : asu(M);
      gstore(ws + G_PS + (size_t)((kpair * 4 + h) * 8 + ks) * 4 + d, (d & 1) ? (u & 0xffffu) : (u >> 16), tag);
    }
  }
  STAMP(7);
  if (ABL_T & 2) {} else if (w == 4 || (DUP1 && w == 6)) {
    if (DUP1 && w == 6) __builtin_amdgcn_s_sleep(DUP1);
    const __amdgpu_buffer_rsrc_t rp = __builtin_amdgcn_make_buffer_rsrc((void*)(ws + G_PX + (size_t)pair * 4096 + 64 * s), 0, 8 * 4096, RSRC_W3);
    const __amdgpu_buffer_rsrc_t rs = __builtin_amdgcn_make_buffer_rsrc((void*)(ws + G_PS + (size_t)(pair * 4 + (s >> 1)) * 32), 0, 256, RSRC_W3);
    const int poff = (lane >> 5) * 4096 + (lane & 31) * 16;
    v4u gp[4]; v2u gs;
#pragma nounroll
    for (unsigned spin = 0; spin < SPIN_CAP; ++spin) {
      asm volatile("" ::: "memory");
      gs = __builtin_amdgcn_raw_buffer_load_b64(rs, (lane & 31) * 8, 0, AUX_SC1);
#pragma unroll
      for (int k = 0; k < 4; ++k) gp[k] = __builtin_amdgcn_raw_buffer_load_b128(rp, poff, k * 8192, AUX_SC1);
      const unsigned bad = gbad2(gp[0], tag) | gbad2(gp[1], tag) | gbad2(gp[2], tag) | gbad2(gp[3], tag) | (gs.y ^ tag.hi) | ((gs.x ^ tag.lo8) & 0xff000000u);
      if (__all(bad == 0u)) break;
      __builtin_amdgcn_s_sleep(PSLEEP);
    }
    STAMP(8);
    const unsigned sv = gs.x & 0xffffu;
    const float Mk = asf((asu(dpp<0x00>(asf(sv))) << 16) | asu(dpp<0x55>(asf(sv))));
    const float Lk = asf((asu(dpp<0xAA>(asf(sv))) << 16) | asu(dpp<0xFF>(asf(sv))));
    float M = fmaxf(Mk, dpp<ROR4>(Mk)); M = fmaxf(M, dpp<ROR8>(M));
    { auto t = __builtin_amdgcn_permlane16_swap(asu(M), asu(M), false, false); M = fmaxf(asf(t[0]), asf(t[1])); }
    const float fk = __builtin_amdgcn_exp2f(Mk - M);
    float L = fk * Lk; L += dpp<ROR4>(L); L += dpp<ROR8>(L); L = tr16(L, L);
    float o0 = 0.f, o1 = 0.f;
#pragma unroll
    for (int k = 0; k < 4; ++k) {
      const float fe = asf(__builtin_amdgcn_readlane(asu(fk), 8 * k)), fo = asf(__builtin_amdgcn_readlane(asu(fk), 8 * k + 4));
      const float f = (lane >> 5) ? fo : fe;
      o0 = fmaf(f, asf(gp[k].x << 8), o0); o1 = fmaf(f, asf(gp[k].z << 8), o1);
    }
    const float ot = tr32(o0, o1);
    const int e = 2 * (lane & 31) + (lane >> 5);
    ((u16*)(lds + L_OV))[(e & 32) + (e & 3) * 8 + ((e >> 2) & 7)] = (u16)f2bf(RCP ? ot * __builtin_amdgcn_rcpf(L) : ot / L);
    lds_set(lb + L_FO, ltag);
  } else if (w < 4) {
    const __amdgpu_buffer_rsrc_t rsx = __builtin_amdgcn_make_buffer_rsrc((void*)(ws + G_SX + ((size_t)(b * 64 + 16 * w) * 64 + cb) * 8), 0, 16 * 4096, RSRC_W3);
    lds_wait1(lb + L_FO, ltag);
    const v4u a0 = *(const v4u*)(lds + L_OV + (lane >> 4) * 16), a1 = *(const v4u*)(lds + L_OV + 64 + (lane >> 4) * 16);
    f32x4 d[8];
#pragma unroll
    for (int jx = 0; jx < 8; ++jx) d[jx] = mfma(a0, bw[0][jx], f32x4{0.f, 0.f, 0.f, 0.f});
#pragma unroll
    for (int jx = 0; jx < 8; ++jx) d[jx] = mfma(a1, bw[1][jx], d[jx]);
    const int g4 = lane >> 4;
    const float v0 = (g4 & 2) ? ((g4 & 1) ? d[6][0] : d[4][0]) : ((g4 & 1) ? d[2][0] : d[0][0]);
    const float v1 = (g4 & 2) ? ((g4 & 1) ? d[7][0] : d[5][0]) : ((g4 & 1) ? d[3][0] : d[1][0]);
    __builtin_amdgcn_raw_buffer_store_b128(v4u{enc24(v0) | tag.lo8, tag.hi, enc24(v1) | tag.lo8, tag.hi}, rsx, (lane & 15) * 4096 + g4 * 16, 0, AUX_SC1);
    STAMP(9);
  } else if (w == 5 && !(ABL_T & 1)) {
    const unsigned b2p = *(const GAS unsigned*)(p.b2 + cb * 8 + 2 * (lane & 3));
    float m0, m1;
    gather8<2>(ws + G_MX + (size_t)(b * 64 + cb) * 256, 28, tag, lane, m0, m1);
    m0 = rbf(rbf(m0) + bflo(b2p)); m1 = rbf(rbf(m1) + bfhi(b2p));
    float2 gt;
    gt.x = rbf(m0 / (1.0f + __expf(-m0))); gt.y = rbf(m1 / (1.0f + __expf(-m1)));
    float s0, s1;
    gather8<4>(ws + G_SX + (size_t)(b * 64 + cb) * 512, 64, tag, lane, s0, s1);
    STAMP(10);
    const float o0 = bflo(xp) + rbf(rbf(s0) * gt.x), o1 = bfhi(xp) + rbf(rbf(s1) * gt.y);
    if (lane < 4) *(GAS unsigned*)(p.out + b * DM + cb * 8 + 2 * lane) = cvtpk(o0, o1);
    if (bid == 0 && lane == 0) __hip_atomic_store((GAS u64*)(ws + G_EPOCH), ((u64)tag.hi << 32) | tag.lo8, RLX_AGENT);
  }
}

}

extern "C" void kernel_launch(void* const* d_in, const int* in_sizes, int n_in,
                              void* d_out, int out_size, void* d_ws, size_t ws_size,
                              hipStream_t stream) {
  Params p;
  p.x = (const u16*)d_in[0]; p.k = (const u16*)d_in[1]; p.v = (const u16*)d_in[2];
  p.wq = (const u16*)d_in[3]; p.wo = (const u16*)d_in[4];
  p.w1 = (const u16*)d_in[6]; p.b1 = (const u16*)d_in[7]; p.w2 = (const u16*)d_in[8]; p.b2 = (const u16*)d_in[9];
  p.out = (u16*)d_out; p.ws = (u64*)d_ws;
  hipLaunchKernelGGL(fused_decode, dim3(NWG), dim3(NTHR), 0, stream, p);
}
```

```cpp
#include <hip/hip_runtime.h>
#include <hip/hip_bf16.h>
#include <stdint.h>

namespace {

typedef unsigned v4u __attribute__((ext_vector_type(4)));
typedef unsigned v2u __attribute__((ext_vector_type(2)));
typedef float f32x4 __attribute__((ext_vector_type(4)));
typedef short bf16x8 __attribute__((ext_vector_type(8)));
typedef unsigned long long u64;
typedef unsigned short u16;
#define GAS __attribute__((address_space(1)))
#define RLX_AGENT __ATOMIC_RELAXED, __HIP_MEMORY_SCOPE_AGENT

constexpr int DM = 512, SK = 16384, FF = 1792;
constexpr int NWG = 256, NTHR = 512;
constexpr int W = 32;
constexpr int RSRC_W3 = 0x00020000;
constexpr int AUX_NT = 2, AUX_SC1 = 16;
#define AUX_W 0
#define AUX_K 2
#define AUX_V 2
#define TOPWIN 0
#define PW 1
#define AUX_W2 0
#define LATEK 0
#define ABL_T 0
#define XMAP 2
#define SKT 0
#define PRET 0
#define LATESIDE 3
#define HSTR 4096
#define DUP1 0
#define RCP 0
#define OUTSC1 1
#define LDSKB 108
#define ABL_RED 0
#define DRY 0
#define AUX_G 16
#define PSLEEP 2
constexpr unsigned SPIN_CAP = 1u << 16;

constexpr size_t G_EPOCH = 0;
constexpr size_t G_QX = 512;
constexpr size_t G_PS = G_QX + 4 * 4096;
constexpr size_t G_PX = G_PS + 32 * 8 * 16;
constexpr size_t G_SX = G_PX + 32 * 8 * 512;
constexpr size_t G_HX = G_SX + 4 * 64 * 64 * 8;
constexpr size_t G_MX = G_HX + 4 * 2048;
constexpr size_t G_END = G_MX + 4 * 64 * 32 * 8;
constexpr size_t G_SHADOW = 524288;
static_assert(G_END <= G_SHADOW, "ws");

constexpr int WO_LD = 1040;
constexpr int L_OM = 0;
constexpr int L_XS = 16384, L_YS = L_XS + 8192;
constexpr int L_RED = 32768, L_REDH = L_RED + 1024, L_QS = L_REDH + 1024, L_SM = L_QS + 1024, L_SL = L_SM + 128,
              L_GATE = L_SL + 128, L_FA = L_GATE + 64, L_FB = L_FA + 16, L_FC = L_FB + 16, L_FD = L_FC + 16,
              L_FQ = L_FD + 16, L_TAG = L_FQ + 16, L_FO = L_TAG + 16, L_FL = L_FO + 16, L_OV = L_FA + 128, L_WO = L_OV + 8 * 256, L_END = L_WO;
constexpr int LDS_BYTES = LDSKB * 1024;
static_assert(L_END <= LDS_BYTES && L_WO % 16 == 0 && L_OV % 16 == 0 && L_FA % 16 == 0, "lds");

struct Params {
  const u16* x; const u16* k; const u16* v; const u16* wq; const u16* wo;
  const u16* w1; const u16* b1; const u16* w2; const u16* b2;
  u16* out; u64* ws;
};

__device__ __forceinline__ float asf(unsigned u) { return __builtin_bit_cast(float, u); }
__device__ __forceinline__ unsigned asu(float f) { return __builtin_bit_cast(unsigned, f); }
__device__ __forceinline__ float bflo(unsigned u) { return asf(u << 16); }
__device__ __forceinline__ float bfhi(unsigned u) { return asf(u & 0xffff0000u); }
__device__ __forceinline__ unsigned f2bf(float f) { return (unsigned)__builtin_bit_cast(u16, (__bf16)f); }
__device__ __forceinline__ float rbf(float f) { return (float)(__bf16)f; }
__device__ __forceinline__ unsigned cvtpk(float lo, float hi) {
  typedef __bf16 bf2 __attribute__((ext_vector_type(2)));
  bf2 r; r.x = (__bf16)lo; r.y = (__bf16)hi;
  return __builtin_bit_cast(unsigned, r);
}
template <int CTRL> __device__ __forceinline__ float dpp(float x) {
  return __builtin_bit_cast(float, __builtin_amdgcn_mov_dpp(__builtin_bit_cast(int, x), CTRL, 0xf, 0xf, true));
}
constexpr int XOR1 = 0xB1, XOR2 = 0x4E, HMIR = 0x141, MIR = 0x140, ROR8 = 0x128, ROR4 = 0x124;
__device__ __forceinline__ float tr32(float a, float b) {
  auto r = __builtin_amdgcn_permlane32_swap(asu(a), asu(b), false, false);
  return asf(r[0]) + asf(r[1]);
}
__device__ __forceinline__ float tr16(float a, float b) {
  auto r = __builtin_amdgcn_permlane16_swap(asu(a), asu(b), false, false);
  return asf(r[0]) + asf(r[1]);
}
__device__ __forceinline__ float max_rows(float x) {
  auto s = __builtin_amdgcn_permlane16_swap(asu(x), asu(x), false, false);
  x = fmaxf(asf(s[0]), asf(s[1]));
  auto t = __builtin_amdgcn_permlane32_swap(asu(x), asu(x), false, false);
  return fmaxf(asf(t[0]), asf(t[1]));
}
__device__ __forceinline__ float sum_rows(float x) { x = tr16(x, x); return tr32(x, x); }
__device__ __forceinline__ float wave_sum(float x) {
  x += dpp<XOR1>(x); x += dpp<XOR2>(x); x += dpp<HMIR>(x); x += dpp<MIR>(x);
  return sum_rows(x);
}
__device__ __forceinline__ float treduce8(const float (&v)[8], int lane) {
  const float u0 = tr32(v[0], v[4]), u1 = tr32(v[1], v[5]), u2 = tr32(v[2], v[6]), u3 = tr32(v[3], v[7]);
  const float w0 = tr16(u0, u2), w1 = tr16(u1, u3);
  const float t0 = w0 + dpp<ROR8>(w0), t1 = w1 + dpp<ROR8>(w1);
  return (lane & 8) ? t1 : t0;
}
__device__ __forceinline__ void ln_row(const float (&v)[8], float (&o)[8]) {
  float s = 0.f;
#pragma unroll
  for (int i = 0; i < 8; ++i) s += v[i];
  s = wave_sum(s);
  const float mean = s * (1.0f / DM);
  float ss = 0.f;
#pragma unroll
  for (int i = 0; i < 8; ++i) { const float d = v[i] - mean; ss = fmaf(d, d, ss); }
  ss = wave_sum(ss);
  const float var_b = rbf(ss * (1.0f / DM)), m_b = rbf(mean);
  const float den = rbf(sqrtf(rbf(var_b + 1e-5f)));
#pragma unroll
  for (int i = 0; i < 8; ++i) o[i] = rbf(rbf(v[i] - m_b) / den);
}
#define WG_BARRIER() asm volatile("s_waitcnt lgkmcnt(0)\n\ts_barrier" ::: "memory")
#define SCHED() __builtin_amdgcn_sched_barrier(0)
#define STAMP(k) do {} while (0)

struct Tag { unsigned lo8, hi; };
__device__ __forceinline__ void gstore(u64* p, unsigned payload24, Tag t) {
  __hip_atomic_store((GAS u64*)p, ((u64)t.hi << 32) | (u64)(payload24 | t.lo8), RLX_AGENT);
}
__device__ __forceinline__ u64 gload(const u64* p) { return __hip_atomic_load((const GAS u64*)p, RLX_AGENT); }
__device__ __forceinline__ bool gok(u64 g, Tag t) {
  return (unsigned)(g >> 32) == t.hi && ((unsigned)g & 0xff000000u) == t.lo8;
}
__device__ __forceinline__ unsigned gbad2(v4u g, Tag t) {
  return (((g.x ^ t.lo8) | (g.z ^ t.lo8)) & 0xff000000u) | (g.y ^ t.hi) | (g.w ^ t.hi);
}
__device__ __forceinline__ bool gok2(v4u g, Tag t) { return gbad2(g, t) == 0u; }
__device__ __forceinline__ unsigned enc24(float f) { unsigned u = asu(f); u += 0x7fu + ((u >> 8) & 1u); return u >> 8; }
__device__ __forceinline__ float dec24(u64 g) { return asf((unsigned)g << 8); }

__device__ __forceinline__ f32x4 mfma(v4u a, v4u b, f32x4 c) {
  return __builtin_amdgcn_mfma_f32_16x16x32_bf16(__builtin_bit_cast(bf16x8, a), __builtin_bit_cast(bf16x8, b), c, 0, 0, 0);
}

__device__ __forceinline__ float gemv16(const unsigned (&wv)[16], const float* vec, int rg, int lane) {
  float acc[8];
#pragma unroll
  for (int bb = 0; bb < 4; ++bb) {
    float a0 = 0.f, a1 = 0.f;
#pragma unroll
    for (int q4 = 0; q4 < 4; ++q4) {
      const f32x4 x0 = *(const f32x4*)(vec + bb * DM + 16 * rg + 4 * q4);
#pragma unroll
      for (int r = 0; r < 4; ++r) { a0 = fmaf(x0[r], bflo(wv[4 * q4 + r]), a0); a1 = fmaf(x0[r], bfhi(wv[4 * q4 + r]), a1); }
    }
    acc[bb * 2] = a0; acc[bb * 2 + 1] = a1;
  }
  return treduce8(acc, lane);
}
__device__ __forceinline__ void lds_set(unsigned addr, unsigned v) {
  asm volatile("s_waitcnt lgkmcnt(0)\n\tds_write_b32 %0, %1" :: "v"(addr), "v"(v) : "memory");
}
__device__ __forceinline__ void lds_wait4(unsigned addr, unsigned v) {
#pragma nounroll
  for (unsigned spin = 0; spin < (1u << 22); ++spin) {
    v4u f;
    asm volatile("ds_read_b128 %0, %1\n\ts_waitcnt lgkmcnt(0)" : "=v"(f) : "v"(addr) : "memory");
    if (__builtin_amdgcn_readfirstlane((f.x == v) & (f.y == v) & (f.z == v) & (f.w == v))) break;
    __builtin_amdgcn_s_sleep(1);
  }
}
__device__ __forceinline__ void lds_wait1(unsigned addr, unsigned v) {
#pragma nounroll
  for (unsigned spin = 0; spin < (1u << 22); ++spin) {
    unsigned f;
    asm volatile("ds_read_b32 %0, %1\n\ts_waitcnt lgkmcnt(0)" : "=v"(f) : "v"(addr) : "memory");
    if (__builtin_amdgcn_readfirstlane(f) == v) break;
    __builtin_amdgcn_s_sleep(1);
  }
}

struct Tile { v4u k[8]; v4u v[8]; };
__device__ __forceinline__ void ld8(v4u (&d)[8], const u16* base, bool valid, int off) {
  const __amdgpu_buffer_rsrc_t r = __builtin_amdgcn_make_buffer_rsrc((void*)base, 0, valid ? HSTR + 4096 : 0, RSRC_W3);
#pragma unroll
  for (int i = 0; i < 8; ++i) d[i] = __builtin_amdgcn_raw_buffer_load_b128(r, off + (i & 3) * 1024, (i >> 2) * HSTR, AUX_NT);
  SCHED();
}
__device__ __forceinline__ void qk_tile(v4u (&k)[8], v4u qf, float (&sc)[8], const u16* kn, bool nvalid, int koff, float bias) {
  constexpr float C2 = 0.08838834764831845f * 1.4426950408889634f;
  f32x4 d[8];
#pragma unroll
  for (int i = 0; i < 8; ++i) d[i] = mfma(k[i], qf, f32x4{0.f, 0.f, 0.f, 0.f});
  SCHED();
  ld8(k, kn, nvalid, koff);
#pragma unroll
  for (int i = 0; i < 8; ++i) sc[i] = fmaf((dpp<0x00>(d[i][0]) + dpp<0x55>(d[i][1])) + (dpp<0xAA>(d[i][2]) + dpp<0xFF>(d[i][3])), C2, bias);
}
__device__ __forceinline__ void pv_tile(v4u (&v)[8], const float (&sc)[8], float& m, float& l, f32x4 (&acc)[8], int lane,
                                        const u16* vn, bool nvalid, int voff, v4u (&k)[8], const u16* kn, int koff) {
  float tm = fmaxf(fmaxf(fmaxf(sc[0], sc[1]), fmaxf(sc[2], sc[3])), fmaxf(fmaxf(sc[4], sc[5]), fmaxf(sc[6], sc[7])));
  tm = max_rows(tm);
  if (__any(tm > m)) {
    const float mn = fmaxf(m, tm), al = __builtin_amdgcn_exp2f(m - mn);
    m = mn; l *= al;
    const float ah = __shfl(al, (lane >> 4) * 4);
#pragma unroll
    for (int j = 0; j < 8; ++j) acc[j] *= ah;
  }
  v4u pk;
#pragma unroll
  for (int k = 0; k < 4; ++k) {
    const float p0 = __builtin_amdgcn_exp2f(sc[2 * k] - m), p1 = __builtin_amdgcn_exp2f(sc[2 * k + 1] - m);
    l += p0 + p1; pk[k] = cvtpk(p0, p1);
  }
#pragma unroll
  for (int jj = 0; jj < 4; ++jj) {
    v4u lo, hi;
#pragma unroll
    for (int k2 = 0; k2 < 4; ++k2) {
      lo[k2] = __builtin_amdgcn_perm(v[2 * k2 + 1][jj], v[2 * k2][jj], 0x05040100u);
      hi[k2] = __builtin_amdgcn_perm(v[2 * k2 + 1][jj], v[2 * k2][jj], 0x07060302u);
    }
    acc[2 * jj] = mfma(pk, lo, acc[2 * jj]); acc[2 * jj + 1] = mfma(pk, hi, acc[2 * jj + 1]);
  }
  SCHED();
  ld8(v, vn, nvalid, voff);
}
template <int NK> __device__ __forceinline__ void poll_pairs(__amdgpu_buffer_rsrc_t r, int voff, int kstride, unsigned skip, Tag tag, v4u (&gv)[NK]) {
  int off[NK];
#pragma unroll
  for (int k = 0; k < NK; ++k) { off[k] = ((skip >> k) & 1u) ? 0x40000000 : voff; gv[k] = v4u{0u, 0u, 0u, 0u}; }
#pragma nounroll
  for (unsigned spin = 0; spin < SPIN_CAP; ++spin) {
    asm volatile("" ::: "memory");
    v4u t[NK];
#pragma unroll
    for (int k = 0; k < NK; ++k) t[k] = __builtin_amdgcn_raw_buffer_load_b128(r, off[k], k * kstride, AUX_SC1);
    bool pend = false;
#pragma unroll
    for (int k = 0; k < NK; ++k) {
      const bool got = gbad2(t[k], tag) == 0u;
      const bool live = off[k] == voff;
      if (got & live) { gv[k].x = t[k].x; gv[k].z = t[k].z; off[k] = 0x40000000; }
      pend |= live & !got;
    }
    if (!__any(pend)) break;
    __builtin_amdgcn_s_sleep(PSLEEP);
  }
}
template <int NK> __device__ __forceinline__ void gather8(const u64* src, int nparts, Tag tag, int lane, float& s0, float& s1) {
  const __amdgpu_buffer_rsrc_t r = __builtin_amdgcn_make_buffer_rsrc((void*)src, 0, NK * 1024, RSRC_W3);
  v4u gv[NK];
  unsigned skip = 0u;
#pragma unroll
  for (int k = 0; k < NK; ++k) skip |= (16 * k + (lane >> 2) >= nparts) ? (1u << k) : 0u;
  poll_pairs<NK>(r, lane * 16, 1024, skip, tag, gv);
  s0 = 0.f; s1 = 0.f;
#pragma unroll
  for (int k = 0; k < NK; ++k) { s0 += asf(gv[k].x << 8); s1 += asf(gv[k].z << 8); }
  s0 += dpp<ROR4>(s0); s1 += dpp<ROR4>(s1);
  s0 += dpp<ROR8>(s0); s1 += dpp<ROR8>(s1);
  s0 = sum_rows(s0); s1 = sum_rows(s1);
}

__global__ void __launch_bounds__(NTHR, 2) fused_decode(Params p) {
  __shared__ __attribute__((aligned(16))) unsigned char lds[LDS_BYTES];
  const int tid = threadIdx.x, lane = tid & 63, w = __builtin_amdgcn_readfirstlane(tid >> 6);
  const int bid = blockIdx.x, g = bid & 7, j = bid >> 3, b = j >> 3, s = j & 7, pair = b * 8 + g, cb = g * 8 + s;
  const int kbt = g >> 1, kg = j >> 2, ks = (j & 3) * 2 + (g & 1);
  const int tile0 = (j & 3) * 128 + ((g & 1) ? 64 - SKT : 0), ntl = (g & 1) ? 64 + SKT : 64 - SKT;
  const int kpair = kbt * 8 + kg, nmine = (ntl - w + 7) >> 3;
  u64* ws = p.ws;
  const unsigned lb = (unsigned)(size_t)(__attribute__((address_space(3))) unsigned char*)lds;
  STAMP(0);

  if (tid < 32) ((unsigned*)(lds + L_FA))[tid] = 0u;
  const size_t kvoff = ((size_t)kpair * SK + tile0 * 32 + w * (HSTR == 4096 ? 32 : 16)) * 128;
  const u16* kb = p.k + kvoff;
  const u16* vb = p.v + kvoff;
  const int koff = ((lane & 15) >> 2) * 256 + (lane & 3) * 16 + (lane >> 4) * 64, voff = lane * 16;
  constexpr unsigned ltag = 1u;
  Tile A, B;
#define ISSUE_WINDOW(on) do { ld8(A.k, kb, on, koff); ld8(A.v, vb, on, voff); ld8(B.k, kb + 32768, on, koff); ld8(B.v, vb + 32768, on, voff); } while (0)
  const int cp = tid & 7, rg = tid >> 3;
  const bool hwg = j >= 18;
  const int f0 = 224 * g + 16 * (hwg ? j - 18 : 0);
  const int ob = lane >> 4, ocol = 2 * (lane & 7) + ((lane >> 3) & 1);
  u64 e_raw; v4u xr; unsigned wqv[16], w1v[16];
  int wA = w, wB = w, wC = w;
  asm volatile("" : "+s"(wA)); asm volatile("" : "+s"(wB)); asm volatile("" : "+s"(wC));
  WG_BARRIER();
  if (wA < 4) {
    e_raw = gload(ws + G_EPOCH);
    xr = *(const GAS v4u*)(p.x + w * DM + 8 * lane);
    const __amdgpu_buffer_rsrc_t rq = __builtin_amdgcn_make_buffer_rsrc((void*)(p.wq + g * 512 + 16 * j), 0, 512 * 8192 - 8192 + 32, RSRC_W3);
#pragma unroll
    for (int r = 0; r < 16; ++r) wqv[r] = __builtin_amdgcn_raw_buffer_load_b32(rq, rg * (16 * 8192) + cp * 4, r * 8192, AUX_W);
    const __amdgpu_buffer_rsrc_t r1 = __builtin_amdgcn_make_buffer_rsrc((void*)(p.w1 + f0), 0, hwg ? 511 * FF * 2 + 32 : 0, RSRC_W3);
#pragma unroll
    for (int r = 0; r < 16; ++r) w1v[r] = __builtin_amdgcn_raw_buffer_load_b32(r1, rg * (16 * FF * 2) + cp * 4, r * FF * 2, AUX_W);
    SCHED();
  }
  if (wC < 4) {
    const unsigned b1v = p.b1[f0 + ocol];
    SCHED();
    float* xs = (float*)(lds + L_XS);
    float* ys = (float*)(lds + L_YS);
    float* red = (float*)(lds + L_RED);
    float* redh = (float*)(lds + L_REDH);
    float xv[8], xn[8];
#pragma unroll
    for (int i = 0; i < 4; ++i) { xv[2 * i] = bflo(xr[i]); xv[2 * i + 1] = bfhi(xr[i]); }
    ln_row(xv, xn);
    *(f32x4*)(xs + w * DM + 8 * lane) = f32x4{xn[0], xn[1], xn[2], xn[3]};
    *(f32x4*)(xs + w * DM + 8 * lane + 4) = f32x4{xn[4], xn[5], xn[6], xn[7]};
    const u64 e_ = ((u64)__builtin_amdgcn_readfirstlane((unsigned)(e_raw >> 32)) << 32) | (u64)__builtin_amdgcn_readfirstlane((unsigned)e_raw);
    const u64 tag40 = ((e_ >> 24) * 0x5DEECE66DULL + 0xBULL) & ((1ULL << 40) - 1);
    Tag tag; tag.lo8 = (unsigned)(tag40 & 0xff) << 24; tag.hi = (unsigned)(tag40 >> 8);
    lds_set(lb + L_FA + 4 * w, ltag);
    STAMP(1);
    lds_wait4(lb + L_FA, ltag);
    red[w * 64 + lane] = gemv16(wqv, xs, rg, lane);
    lds_set(lb + L_FB + 4 * w, ltag);
    const __amdgpu_buffer_rsrc_t rqx = __builtin_amdgcn_make_buffer_rsrc((void*)(ws + G_QX + kbt * 4096 + kg * 512), 0, 4096, RSRC_W3);
    v4u gq[4];
    if (w == 0) {
      lds_wait4(lb + L_FB, ltag);
      const float q = (red[lane] + red[64 + lane]) + (red[128 + lane] + red[192 + lane]);
      gstore(ws + G_QX + ob * 4096 + g * 512 + 16 * j + ocol, f2bf(q), tag);
      STAMP(2);
    }
    if (hwg) {
      float y[8];
      ln_row(xn, y);
      *(f32x4*)(ys + w * DM + 8 * lane) = f32x4{y[0], y[1], y[2], y[3]};
      *(f32x4*)(ys + w * DM + 8 * lane + 4) = f32x4{y[4], y[5], y[6], y[7]};
      lds_set(lb + L_FC + 4 * w, ltag);
      lds_wait4(lb + L_FC, ltag);
      redh[w * 64 + lane] = gemv16(w1v, ys, rg, lane);
      lds_set(lb + L_FD + 4 * w, ltag);
      if (w == 1) {
        lds_wait4(lb + L_FD, ltag);
        const float hp = (redh[lane] + redh[64 + lane]) + (redh[128 + lane] + redh[192 + lane]);
        const float pre = rbf(rbf(hp) + bflo(b1v));
        const float hv = 0.5f * pre * (1.0f + tanhf(0.7978845608028654f * (pre + 0.044715f * pre * pre * pre)));
        gstore(ws + G_HX + ob * 2048 + f0 + ocol, f2bf(hv), tag);
      }
    }
    if (w == 0) {
      poll_pairs<4>(rqx, lane * 16, 1024, 0u, tag, gq);
#pragma unroll
      for (int k = 0; k < 4; ++k) ((unsigned*)(lds + L_QS))[64 * k + lane] = (gq[k].x & 0xffffu) | (gq[k].z << 16);
      *(v2u*)(lds + L_TAG) = v2u{tag.lo8, tag.hi};
      lds_set(lb + L_FQ, ltag);
      STAMP(3);
    }
  }
  SCHED();
  ISSUE_WINDOW(true);
  lds_wait1(lb + L_FQ, ltag);
  Tag tag;
  { const v2u t = *(const v2u*)(lds + L_TAG); tag.lo8 = __builtin_amdgcn_readfirstlane(t.x); tag.hi = __builtin_amdgcn_readfirstlane(t.y); }
  STAMP(4);
  const v4u qf = *(const v4u*)(lds + L_QS + ((lane & 15) >> 2) * 256 + ((lane & 3) + 4 * (lane >> 4)) * 16);

  float m = -INFINITY, lsum = 0.f;
  f32x4 acc[8];
#pragma unroll
  for (int i = 0; i < 8; ++i) acc[i] = f32x4{0.f, 0.f, 0.f, 0.f};
  float sA[8], sB[8];
#pragma nounroll
  for (int t = 0; t < nmine; t += 2) {
    const bool na = t + 2 < nmine, nb = t + 3 < nmine;
    if (w >= 4 && t + 2 * LATESIDE >= nmine) lds_set(lb + L_FL + 4 * (w - 4), ltag);
    const size_t oa = (size_t)(na ? t + 2 : 0) * 32768, ob2 = (size_t)(nb ? t + 3 : 0) * 32768;
    qk_tile(A.k, qf, sA, kb + oa, na, koff, 0.f);
    pv_tile(A.v, sA, m, lsum, acc, lane, vb + oa, na, voff, A.k, kb + oa, koff);
    qk_tile(B.k, qf, sB, kb + ob2, nb, koff, t + 1 < nmine ? 0.f : -INFINITY);
    pv_tile(B.v, sB, m, lsum, acc, lane, vb + ob2, nb, voff, B.k, kb + ob2, koff);
  }
  lsum = sum_rows(lsum);
  STAMP(5);
  float* om = (float*)(lds + L_OM);
  float* sm = (float*)(lds + L_SM);
  float* sl = (float*)(lds + L_SL);
  *(f32x4*)(om + w * 512 + lane * 8) = f32x4{acc[0][0], acc[1][0], acc[2][0], acc[3][0]};
  *(f32x4*)(om + w * 512 + lane * 8 + 4) = f32x4{acc[4][0], acc[5][0], acc[6][0], acc[7][0]};
  if (lane < 16 && (lane & 3) == 0) { sm[(lane >> 2) * 8 + w] = m; sl[(lane >> 2) * 8 + w] = lsum; }
  const int r8 = lane >> 3, c8 = lane & 7;

  v4u bw[2][8];
  if (w < 4) {
    lds_wait4(lb + L_FL, ltag);
    v4u t16[16];
    const __amdgpu_buffer_rsrc_t rwo = __builtin_amdgcn_make_buffer_rsrc((void*)(p.wo + (size_t)(g * 512 + 64 * s) * DM + 128 * w), 0, ((ABL_RED & 1) || ((ABL_RED & 8) && w != b)) ? 0 : 63 * 1024 + 256, RSRC_W3);
#pragma unroll
    for (int r = 0; r < 16; ++r) t16[r] = __builtin_amdgcn_raw_buffer_load_b128(rwo, (lane >> 4) * 1024 + (lane & 15) * 16, r * 4096, AUX_W2);
    v4u w2v[8];
    const int f0 = 256 * g + 64 * w;
    const __amdgpu_buffer_rsrc_t rw2 = __builtin_amdgcn_make_buffer_rsrc((void*)(p.w2 + (size_t)(g < 7 ? f0 : 0) * DM + 64 * s), 0, (g < 7 && !(ABL_RED & 2) && !((ABL_RED & 16) && w != b)) ? 63 * 1024 + 128 : 0, RSRC_W3);
#pragma unroll
    for (int i = 0; i < 8; ++i) w2v[i] = __builtin_amdgcn_raw_buffer_load_b128(rw2, r8 * 1024 + c8 * 16, i * 8192, AUX_W2);
    const u64* hsrc = ws + G_HX + b * 2048 + f0 + lane;
    u64 hg = gload(hsrc);
    SCHED();
#pragma unroll
    for (int ks = 0; ks < 2; ++ks)
#pragma unroll
      for (int jj = 0; jj < 4; ++jj)
#pragma unroll
        for (int k2 = 0; k2 < 4; ++k2) {
          bw[ks][2 * jj][k2] = __builtin_amdgcn_perm(t16[8 * ks + 2 * k2 + 1][jj], t16[8 * ks + 2 * k2][jj], 0x05040100u);
          bw[ks][2 * jj + 1][k2] = __builtin_amdgcn_perm(t16[8 * ks + 2 * k2 + 1][jj], t16[8 * ks + 2 * k2][jj], 0x07060302u);
        }
    if (g < 7 && !(ABL_RED & 4)) {
      if (!__all(gok(hg, tag))) {
        unsigned spin = 0;
        do { __builtin_amdgcn_s_sleep(PSLEEP); hg = gload(hsrc); } while (!__all(gok(hg, tag)) && ++spin < SPIN_CAP);
      }
      const float hl = bflo((unsigned)hg);
      float ma[8];
#pragma unroll
      for (int e = 0; e < 8; ++e) ma[e] = 0.f;
#pragma unroll
      for (int i = 0; i < 8; ++i) {
        const float hv = __shfl(hl, 8 * i + r8);
#pragma unroll
        for (int e = 0; e < 4; ++e) { ma[2 * e] = fmaf(hv, bflo(w2v[i][e]), ma[2 * e]); ma[2 * e + 1] = fmaf(hv, bfhi(w2v[i][e]), ma[2 * e + 1]); }
      }
      const float r = treduce8(ma, lane);
      gstore(ws + G_MX + ((size_t)(b * 64 + 8 * s + c8) * 32 + 4 * g + w) * 8 + r8, enc24(r), tag);
    }
  }
  STAMP(6);
  const unsigned xp = *(const GAS unsigned*)(p.x + b * DM + cb * 8 + 2 * (lane & 3));
  WG_BARRIER();
  {
    const int h = tid >> 7;
    const f32x4 m0 = *(const f32x4*)(sm + h * 8), m1 = *(const f32x4*)(sm + h * 8 + 4);
    float ov[8];
#pragma unroll
    for (int ww = 0; ww < 8; ++ww) ov[ww] = om[ww * 512 + tid];
    const f32x4 l0 = *(const f32x4*)(sl + h * 8), l1 = *(const f32x4*)(sl + h * 8 + 4);
    SCHED();
    const float M = fmaxf(fmaxf(fmaxf(m0[0], m0[1]), fmaxf(m0[2], m0[3])), fmaxf(fmaxf(m1[0], m1[1]), fmaxf(m1[2], m1[3])));
    float O = 0.f, L = 0.f;
#pragma unroll
    for (int ww = 0; ww < 4; ++ww) { const float f = __builtin_amdgcn_exp2f(m0[ww] - M); O = fmaf(f, ov[ww], O); L = fmaf(f, l0[ww], L); }
#pragma unroll
    for (int ww = 0; ww < 4; ++ww) { const float f = __builtin_amdgcn_exp2f(m1[ww] - M); O = fmaf(f, ov[4 + ww], O); L = fmaf(f, l1[ww], L); }
    gstore(ws + G_PX + (size_t)(kpair * 8 + ks) * 512 + tid, enc24(O), tag);
    const int d = tid & 127;
    if (d < 4) {
      const unsigned u = (d & 2) ? asu(L) : asu(M);
      gstore(ws + G_PS + (size_t)((kpair * 4 + h) * 8 + ks) * 4 + d, (d & 1) ? (u & 0xffffu) : (u >> 16), tag);
    }
  }
  STAMP(7);
  if (ABL_T & 2) {} else if (w == 4 || (DUP1 && w == 6)) {
    if (DUP1 && w == 6) __builtin_amdgcn_s_sleep(DUP1);
    const __amdgpu_buffer_rsrc_t rp = __builtin_amdgcn_make_buffer_rsrc((void*)(ws + G_PX + (size_t)pair * 4096 + 64 * s), 0, 8 * 4096, RSRC_W3);
    const __amdgpu_buffer_rsrc_t rs = __builtin_amdgcn_make_buffer_rsrc((void*)(ws + G_PS + (size_t)(pair * 4 + (s >> 1)) * 32), 0, 256, RSRC_W3);
    const int poff = (lane >> 5) * 4096 + (lane & 31) * 16;
    v4u gp[4]; v2u gs;
#pragma nounroll
    for (unsigned spin = 0; spin < SPIN_CAP; ++spin) {
      asm volatile("" ::: "memory");
      gs = __builtin_amdgcn_raw_buffer_load_b64(rs, (lane & 31) * 8, 0, AUX_SC1);
#pragma unroll
      for (int k = 0; k < 4; ++k) gp[k] = __builtin_amdgcn_raw_buffer_load_b128(rp, poff, k * 8192, AUX_SC1);
      const unsigned bad = gbad2(gp[0], tag) | gbad2(gp[1], tag) | gbad2(gp[2], tag) | gbad2(gp[3], tag) | (gs.y ^ tag.hi) | ((gs.x ^ tag.lo8) & 0xff000000u);
      if (__all(bad == 0u)) break;
      __builtin_amdgcn_s_sleep(PSLEEP);
    }
    STAMP(8);
    const unsigned sv = gs.x & 0xffffu;
    const float Mk = asf((asu(dpp<0x00>(asf(sv))) << 16) | asu(dpp<0x55>(asf(sv))));
    const float Lk = asf((asu(dpp<0xAA>(asf(sv))) << 16) | asu(dpp<0xFF>(asf(sv))));
    float M = fmaxf(Mk, dpp<ROR4>(Mk)); M = fmaxf(M, dpp<ROR8>(M));
    { auto t = __builtin_amdgcn_permlane16_swap(asu(M), asu(M), false, false); M = fmaxf(asf(t[0]), asf(t[1])); }
    const float fk = __builtin_amdgcn_exp2f(Mk - M);
    float L = fk * Lk; L += dpp<ROR4>(L); L += dpp<ROR8>(L); L = tr16(L, L);
    float o0 = 0.f, o1 = 0.f;
#pragma unroll
    for (int k = 0; k < 4; ++k) {
      const float fe = asf(__builtin_amdgcn_readlane(asu(fk), 8 * k)), fo = asf(__builtin_amdgcn_readlane(asu(fk), 8 * k + 4));
      const float f = (lane >> 5) ? fo : fe;
      o0 = fmaf(f, asf(gp[k].x << 8), o0); o1 = fmaf(f, asf(gp[k].z << 8), o1);
    }
    const float ot = tr32(o0, o1);
    const int e = 2 * (lane & 31) + (lane >> 5);
    ((u16*)(lds + L_OV))[(e & 32) + (e & 3) * 8 + ((e >> 2) & 7)] = (u16)f2bf(RCP ? ot * __builtin_amdgcn_rcpf(L) : ot / L);
    lds_set(lb + L_FO, ltag);
  } else if (w < 4) {
    const __amdgpu_buffer_rsrc_t rsx = __builtin_amdgcn_make_buffer_rsrc((void*)(ws + G_SX + ((size_t)(b * 64 + 16 * w) * 64 + cb) * 8), 0, 16 * 4096, RSRC_W3);
    lds_wait1(lb + L_FO, ltag);
    const v4u a0 = *(const v4u*)(lds + L_OV + (lane >> 4) * 16), a1 = *(const v4u*)(lds + L_OV + 64 + (lane >> 4) * 16);
    f32x4 d[8];
#pragma unroll
    for (int jx = 0; jx < 8; ++jx) d[jx] = mfma(a0, bw[0][jx], f32x4{0.f, 0.f, 0.f, 0.f});
#pragma unroll
    for (int jx = 0; jx < 8; ++jx) d[jx] = mfma(a1, bw[1][jx], d[jx]);
    const int g4 = lane >> 4;
    const float v0 = (g4 & 2) ? ((g4 & 1) ? d[6][0] : d[4][0]) : ((g4 & 1) ? d[2][0] : d[0][0]);
    const float v1 = (g4 & 2) ? ((g4 & 1) ? d[7][0] : d[5][0]) : ((g4 & 1) ? d[3][0] : d[1][0]);
    __builtin_amdgcn_raw_buffer_store_b128(v4u{enc24(v0) | tag.lo8, tag.hi, enc24(v1) | tag.lo8, tag.hi}, rsx, (lane & 15) * 4096 + g4 * 16, 0, AUX_SC1);
    STAMP(9);
  } else if (w == 5 && !(ABL_T & 1)) {
    const unsigned b2p = *(const GAS unsigned*)(p.b2 + cb * 8 + 2 * (lane & 3));
    float m0, m1;
    gather8<2>(ws + G_MX + (size_t)(b * 64 + cb) * 256, 28, tag, lane, m0, m1);
    m0 = rbf(rbf(m0) + bflo(b2p)); m1 = rbf(rbf(m1) + bfhi(b2p));
    float2 gt;
    gt.x = rbf(m0 / (1.0f + __expf(-m0))); gt.y = rbf(m1 / (1.0f + __expf(-m1)));
    float s0, s1;
    gather8<4>(ws + G_SX + (size_t)(b * 64 + cb) * 512, 64, tag, lane, s0, s1);
    STAMP(10);
    const float o0 = bflo(xp) + rbf(rbf(s0) * gt.x), o1 = bfhi(xp) + rbf(rbf(s1) * gt.y);
    if (lane < 4) __hip_atomic_store((GAS unsigned*)(p.out + b * DM + cb * 8 + 2 * lane), cvtpk(o0, o1), RLX_AGENT);
    if (bid == 0 && lane == 0) __hip_atomic_store((GAS u64*)(ws + G_EPOCH), ((u64)tag.hi << 32) | tag.lo8, RLX_AGENT);
  }
}

}

extern "C" void kernel_launch(void* const* d_in, const int* in_sizes, int n_in,
                              void* d_out, int out_size, void* d_ws, size_t ws_size,
                              hipStream_t stream) {
  Params p;
  p.x = (const u16*)d_in[0]; p.k = (const u16*)d_in[1]; p.v = (const u16*)d_in[2];
  p.wq = (const u16*)d_in[3]; p.wo = (const u16*)d_in[4];
  p.w1 = (const u16*)d_in[6]; p.b1 = (const u16*)d_in[7]; p.w2 = (const u16*)d_in[8]; p.b2 = (const u16*)d_in[9];
  p.out = (u16*)d_out; p.ws = (u64*)d_ws;
  hipLaunchKernelGGL(fused_decode, dim3(NWG), dim3(NTHR), 0, stream, p);
}
```

```cpp
#include <hip/hip_runtime.h>
#include <hip/hip_bf16.h>
#include <stdint.h>

namespace {

typedef unsigned v4u __attribute__((ext_vector_type(4)));
typedef unsigned v2u __attribute__((ext_vector_type(2)));
typedef float f32x4 __attribute__((ext_vector_type(4)));
typedef short bf16x8 __attribute__((ext_vector_type(8)));
typedef unsigned long long u64;
typedef unsigned short u16;
#define GAS __attribute__((address_space(1)))
#define RLX_AGENT __ATOMIC_RELAXED, __HIP_MEMORY_SCOPE_AGENT

constexpr int DM = 512, SK = 16384, FF = 1792;
constexpr int NWG = 256, NTHR = 512;
constexpr int W = 32;
constexpr int RSRC_W3 = 0x00020000;
constexpr int AUX_NT = 2, AUX_SC1 = 16;
#define AUX_W 0
#define AUX_K 2
#define AUX_V 2
#define TOPWIN 0
#define PW 1
#define AUX_W2 0
#define LATEK 0
#define ABL_T 0
#define XMAP 2
#define SKT 0
#define PRET 0
#define LATESIDE 3
#define HSTR 4096
#define DUP1 0
#define RCP 0
#define OUTSC1 0
#define LDSKB 40
#define ROT 0
#define ABL_RED 0
#define DRY 0
#define AUX_G 16
#define PSLEEP 2
constexpr unsigned SPIN_CAP = 1u << 16;

constexpr size_t G_EPOCH = 0;
constexpr size_t G_QX = 512;
constexpr size_t G_PS = G_QX + 4 * 4096;
constexpr size_t G_PX = G_PS + 32 * 8 * 16;
constexpr size_t G_SX = G_PX + 32 * 8 * 512;
constexpr size_t G_HX = G_SX + 4 * 64 * 64 * 8;
constexpr size_t G_MX = G_HX + 4 * 2048;
constexpr size_t G_END = G_MX + 4 * 64 * 32 * 8;
constexpr size_t G_SHADOW = 524288;
static_assert(G_END <= G_SHADOW, "ws");

constexpr int WO_LD = 1040;
constexpr int L_OM = 0;
constexpr int L_XS = 16384, L_YS = L_XS + 8192;
constexpr int L_RED = 32768, L_REDH = L_RED + 1024, L_QS = L_REDH + 1024, L_SM = L_QS + 1024, L_SL = L_SM + 128,
              L_GATE = L_SL + 128, L_FA = L_GATE + 64, L_FB = L_FA + 16, L_FC = L_FB + 16, L_FD = L_FC + 16,
              L_FQ = L_FD + 16, L_TAG = L_FQ + 16, L_FO = L_TAG + 16, L_FL = L_FO + 16, L_OV = L_FA + 128, L_WO = L_OV + 8 * 256, L_END = L_WO;
constexpr int LDS_BYTES = LDSKB * 1024;
static_assert(L_END <= LDS_BYTES && L_WO % 16 == 0 && L_OV % 16 == 0 && L_FA % 16 == 0, "lds");

struct Params {
  const u16* x; const u16* k; const u16* v; const u16* wq; const u16* wo;
  const u16* w1; const u16* b1; const u16* w2; const u16* b2;
  u16* out; u64* ws;
};

__device__ __forceinline__ float asf(unsigned u) { return __builtin_bit_cast(float, u); }
__device__ __forceinline__ unsigned asu(float f) { return __builtin_bit_cast(unsigned, f); }
__device__ __forceinline__ float bflo(unsigned u) { return asf(u << 16); }
__device__ __forceinline__ float bfhi(unsigned u) { return asf(u & 0xffff0000u); }
__device__ __forceinline__ unsigned f2bf(float f) { return (unsigned)__builtin_bit_cast(u16, (__bf16)f); }
__device__ __forceinline__ float rbf(float f) { return (float)(__bf16)f; }
__device__ __forceinline__ unsigned cvtpk(float lo, float hi) {
  typedef __bf16 bf2 __attribute__((ext_vector_type(2)));
  bf2 r; r.x = (__bf16)lo; r.y = (__bf16)hi;
  return __builtin_bit_cast(unsigned, r);
}
template <int CTRL> __device__ __forceinline__ float dpp(float x) {
  return __builtin_bit_cast(float, __builtin_amdgcn_mov_dpp(__builtin_bit_cast(int, x), CTRL, 0xf, 0xf, true));
}
constexpr int XOR1 = 0xB1, XOR2 = 0x4E, HMIR = 0x141, MIR = 0x140, ROR8 = 0x128, ROR4 = 0x124;
__device__ __forceinline__ float tr32(float a, float b) {
  auto r = __builtin_amdgcn_permlane32_swap(asu(a), asu(b), false, false);
  return asf(r[0]) + asf(r[1]);
}
__device__ __forceinline__ float tr16(float a, float b) {
  auto r = __builtin_amdgcn_permlane16_swap(asu(a), asu(b), false, false);
  return asf(r[0]) + asf(r[1]);
}
__device__ __forceinline__ float max_rows(float x) {
  auto s = __builtin_amdgcn_permlane16_swap(asu(x), asu(x), false, false);
  x = fmaxf(asf(s[0]), asf(s[1]));
  auto t = __builtin_amdgcn_permlane32_swap(asu(x), asu(x), false, false);
  return fmaxf(asf(t[0]), asf(t[1]));
}
__device__ __forceinline__ float sum_rows(float x) { x = tr16(x, x); return tr32(x, x); }
__device__ __forceinline__ float wave_sum(float x) {
  x += dpp<XOR1>(x); x += dpp<XOR2>(x); x += dpp<HMIR>(x); x += dpp<MIR>(x);
  return sum_rows(x);
}
__device__ __forceinline__ float treduce8(const float (&v)[8], int lane) {
  const float u0 = tr32(v[0], v[4]), u1 = tr32(v[1], v[5]), u2 = tr32(v[2], v[6]), u3 = tr32(v[3], v[7]);
  const float w0 = tr16(u0, u2), w1 = tr16(u1, u3);
  const float t0 = w0 + dpp<ROR8>(w0), t1 = w1 + dpp<ROR8>(w1);
  return (lane & 8) ? t1 : t0;
}
__device__ __forceinline__ void ln_row(const float (&v)[8], float (&o)[8]) {
  float s = 0.f;
#pragma unroll
  for (int i = 0; i < 8; ++i) s += v[i];
  s = wave_sum(s);
  const float mean = s * (1.0f / DM);
  float ss = 0.f;
#pragma unroll
  for (int i = 0; i < 8; ++i) { const float d = v[i] - mean; ss = fmaf(d, d, ss); }
  ss = wave_sum(ss);
  const float var_b = rbf(ss * (1.0f / DM)), m_b = rbf(mean);
  const float den = rbf(sqrtf(rbf(var_b + 1e-5f)));
#pragma unroll
  for (int i = 0; i < 8; ++i) o[i] = rbf(rbf(v[i] - m_b) / den);
}
#define WG_BARRIER() asm volatile("s_waitcnt lgkmcnt(0)\n\ts_barrier" ::: "memory")
#define SCHED() __builtin_amdgcn_sched_barrier(0)
#define STAMP(k) do {} while (0)

struct Tag { unsigned lo8, hi; };
__device__ __forceinline__ void gstore(u64* p, unsigned payload24, Tag t) {
  __hip_atomic_store((GAS u64*)p, ((u64)t.hi << 32) | (u64)(payload24 | t.lo8), RLX_AGENT);
}
__device__ __forceinline__ u64 gload(const u64* p) { return __hip_atomic_load((const GAS u64*)p, RLX_AGENT); }
__device__ __forceinline__ bool gok(u64 g, Tag t) {
  return (unsigned)(g >> 32) == t.hi && ((unsigned)g & 0xff000000u) == t.lo8;
}
__device__ __forceinline__ unsigned gbad2(v4u g, Tag t) {
  return (((g.x ^ t.lo8) | (g.z ^ t.lo8)) & 0xff000000u) | (g.y ^ t.hi) | (g.w ^ t.hi);
}
__device__ __forceinline__ bool gok2(v4u g, Tag t) { return gbad2(g, t) == 0u; }
__device__ __forceinline__ unsigned enc24(float f) { unsigned u = asu(f); u += 0x7fu + ((u >> 8) & 1u); return u >> 8; }
__device__ __forceinline__ float dec24(u64 g) { return asf((unsigned)g << 8); }

__device__ __forceinline__ f32x4 mfma(v4u a, v4u b, f32x4 c) {
  return __builtin_amdgcn_mfma_f32_16x16x32_bf16(__builtin_bit_cast(bf16x8, a), __builtin_bit_cast(bf16x8, b), c, 0, 0, 0);
}

__device__ __forceinline__ float gemv16(const unsigned (&wv)[16], const float* vec, int rg, int lane) {
  float acc[8];
#pragma unroll
  for (int bb = 0; bb < 4; ++bb) {
    float a0 = 0.f, a1 = 0.f;
#pragma unroll
    for (int q4 = 0; q4 < 4; ++q4) {
      const f32x4 x0 = *(const f32x4*)(vec + bb * DM + 16 * rg + 4 * q4);
#pragma unroll
      for (int r = 0; r < 4; ++r) { a0 = fmaf(x0[r], bflo(wv[4 * q4 + r]), a0); a1 = fmaf(x0[r], bfhi(wv[4 * q4 + r]), a1); }
    }
    acc[bb * 2] = a0; acc[bb * 2 + 1] = a1;
  }
  return treduce8(acc, lane);
}
__device__ __forceinline__ void lds_set(unsigned addr, unsigned v) {
  asm volatile("s_waitcnt lgkmcnt(0)\n\tds_write_b32 %0, %1" :: "v"(addr), "v"(v) : "memory");
}
__device__ __forceinline__ void lds_wait4(unsigned addr, unsigned v) {
#pragma nounroll
  for (unsigned spin = 0; spin < (1u << 22); ++spin) {
    v4u f;
    asm volatile("ds_read_b128 %0, %1\n\ts_waitcnt lgkmcnt(0)" : "=v"(f) : "v"(addr) : "memory");
    if (__builtin_amdgcn_readfirstlane((f.x == v) & (f.y == v) & (f.z == v) & (f.w == v))) break;
    __builtin_amdgcn_s_sleep(1);
  }
}
__device__ __forceinline__ void lds_wait1(unsigned addr, unsigned v) {
#pragma nounroll
  for (unsigned spin = 0; spin < (1u << 22); ++spin) {
    unsigned f;
    asm volatile("ds_read_b32 %0, %1\n\ts_waitcnt lgkmcnt(0)" : "=v"(f) : "v"(addr) : "memory");
    if (__builtin_amdgcn_readfirstlane(f) == v) break;
    __builtin_amdgcn_s_sleep(1);
  }
}

struct Tile { v4u k[8]; v4u v[8]; };
__device__ __forceinline__ void ld8(v4u (&d)[8], const u16* base, bool valid, int off) {
  const __amdgpu_buffer_rsrc_t r = __builtin_amdgcn_make_buffer_rsrc((void*)base, 0, valid ? HSTR + 4096 : 0, RSRC_W3);
#pragma unroll
  for (int i = 0; i < 8; ++i) d[i] = __builtin_amdgcn_raw_buffer_load_b128(r, off + (i & 3) * 1024, (i >> 2) * HSTR, AUX_NT);
  SCHED();
}
__device__ __forceinline__ void qk_tile(v4u (&k)[8], v4u qf, float (&sc)[8], const u16* kn, bool nvalid, int koff, float bias) {
  constexpr float C2 = 0.08838834764831845f * 1.4426950408889634f;
  f32x4 d[8];
#pragma unroll
  for (int i = 0; i < 8; ++i) d[i] = mfma(k[i], qf, f32x4{0.f, 0.f, 0.f, 0.f});
  SCHED();
  ld8(k, kn, nvalid, koff);
#pragma unroll
  for (int i = 0; i < 8; ++i) sc[i] = fmaf((dpp<0x00>(d[i][0]) + dpp<0x55>(d[i][1])) + (dpp<0xAA>(d[i][2]) + dpp<0xFF>(d[i][3])), C2, bias);
}
__device__ __forceinline__ void pv_tile(v4u (&v)[8], const float (&sc)[8], float& m, float& l, f32x4 (&acc)[8], int lane,
                                        const u16* vn, bool nvalid, int voff, v4u (&k)[8], const u16* kn, int koff) {
  float tm = fmaxf(fmaxf(fmaxf(sc[0], sc[1]), fmaxf(sc[2], sc[3])), fmaxf(fmaxf(sc[4], sc[5]), fmaxf(sc[6], sc[7])));
  tm = max_rows(tm);
  if (__any(tm > m)) {
    const float mn = fmaxf(m, tm), al = __builtin_amdgcn_exp2f(m - mn);
    m = mn; l *= al;
    const float ah = __shfl(al, (lane >> 4) * 4);
#pragma unroll
    for (int j = 0; j < 8; ++j) acc[j] *= ah;
  }
  v4u pk;
#pragma unroll
  for (int k = 0; k < 4; ++k) {
    const float p0 = __builtin_amdgcn_exp2f(sc[2 * k] - m), p1 = __builtin_amdgcn_exp2f(sc[2 * k + 1] - m);
    l += p0 + p1; pk[k] = cvtpk(p0, p1);
  }
#pragma unroll
  for (int jj = 0; jj < 4; ++jj) {
    v4u lo, hi;
#pragma unroll
    for (int k2 = 0; k2 < 4; ++k2) {
      lo[k2] = __builtin_amdgcn_perm(v[2 * k2 + 1][jj], v[2 * k2][jj], 0x05040100u);
      hi[k2] = __builtin_amdgcn_perm(v[2 * k2 + 1][jj], v[2 * k2][jj], 0x07060302u);
    }
    acc[2 * jj] = mfma(pk, lo, acc[2 * jj]); acc[2 * jj + 1] = mfma(pk, hi, acc[2 * jj + 1]);
  }
  SCHED();
  ld8(v, vn, nvalid, voff);
}
template <int NK> __device__ __forceinline__ void poll_pairs(__amdgpu_buffer_rsrc_t r, int voff, int kstride, unsigned skip, Tag tag, v4u (&gv)[NK]) {
  int off[NK];
#pragma unroll
  for (int k = 0; k < NK; ++k) { off[k] = ((skip >> k) & 1u) ? 0x40000000 : voff; gv[k] = v4u{0u, 0u, 0u, 0u}; }
#pragma nounroll
  for (unsigned spin = 0; spin < SPIN_CAP; ++spin) {
    asm volatile("" ::: "memory");
    v4u t[NK];
#pragma unroll
    for (int k = 0; k < NK; ++k) t[k] = __builtin_amdgcn_raw_buffer_load_b128(r, off[k], k * kstride, AUX_SC1);
    bool pend = false;
#pragma unroll
    for (int k = 0; k < NK; ++k) {
      const bool got = gbad2(t[k], tag) == 0u;
      const bool live = off[k] == voff;
      if (got & live) { gv[k].x = t[k].x; gv[k].z = t[k].z; off[k] = 0x40000000; }
      pend |= live & !got;
    }
    if (!__any(pend)) break;
    __builtin_amdgcn_s_sleep(PSLEEP);
  }
}
template <int NK> __device__ __forceinline__ void gather8(const u64* src, int nparts, Tag tag, int lane, float& s0, float& s1) {
  const __amdgpu_buffer_rsrc_t r = __builtin_amdgcn_make_buffer_rsrc((void*)src, 0, NK * 1024, RSRC_W3);
  v4u gv[NK];
  unsigned skip = 0u;
#pragma unroll
  for (int k = 0; k < NK; ++k) skip |= (16 * k + (lane >> 2) >= nparts) ? (1u << k) : 0u;
  poll_pairs<NK>(r, lane * 16, 1024, skip, tag, gv);
  s0 = 0.f; s1 = 0.f;
#pragma unroll
  for (int k = 0; k < NK; ++k) { s0 += asf(gv[k].x << 8); s1 += asf(gv[k].z << 8); }
  s0 += dpp<ROR4>(s0); s1 += dpp<ROR4>(s1);
  s0 += dpp<ROR8>(s0); s1 += dpp<ROR8>(s1);
  s0 = sum_rows(s0); s1 = sum_rows(s1);
}

__global__ void __launch_bounds__(NTHR, 2) fused_decode(Params p) {
  __shared__ __attribute__((aligned(16))) unsigned char lds[LDS_BYTES];
  const int tid = threadIdx.x, lane = tid & 63, w = __builtin_amdgcn_readfirstlane(tid >> 6);
  const int bid = blockIdx.x, g = bid & 7, j = bid >> 3, b = j >> 3, s = j & 7, pair = b * 8 + g, cb = g * 8 + s;
  const int kbt = g >> 1, kg = j >> 2, ks = (j & 3) * 2 + (g & 1);
  const int tile0 = (j & 3) * 128 + ((g & 1) ? 64 - SKT : 0), ntl = (g & 1) ? 64 + SKT : 64 - SKT;
  const int kpair = kbt * 8 + kg, nmine = (ntl - w + 7) >> 3;
  u64* ws = p.ws;
  const unsigned lb = (unsigned)(size_t)(__attribute__((address_space(3))) unsigned char*)lds;
  STAMP(0);

  if (tid < 32) ((unsigned*)(lds + L_FA))[tid] = 0u;
  const size_t kvoff = ((size_t)kpair * SK + tile0 * 32 + w * (HSTR == 4096 ? 32 : 16)) * 128;
  const u16* kb = p.k + kvoff;
  const u16* vb = p.v + kvoff;
  const int koff = ((lane & 15) >> 2) * 256 + (lane & 3) * 16 + (lane >> 4) * 64, voff = lane * 16;
  constexpr unsigned ltag = 1u;
  Tile A, B;
  const int rot = ROT == 0 ? 0 : (ROT == 1 ? 2 * ks : (ROT == 2 ? 6 * ks : (ROT == 3 ? (j >> 2) + g : (ROT == 4 ? 6 * ks + 7 * kg + 2 * kbt : 2 * ks + 2 * kbt))));
#define TOFF(t) ((size_t)(ROT ? (((t) + rot) & 7) : (t)) * 32768)
#define ISSUE_WINDOW(on) do { ld8(A.k, kb + TOFF(0), on, koff); ld8(A.v, vb + TOFF(0), on, voff); ld8(B.k, kb + TOFF(1), on, koff); ld8(B.v, vb + TOFF(1), on, voff); } while (0)
  const int cp = tid & 7, rg = tid >> 3;
  const bool hwg = j >= 18;
  const int f0 = 224 * g + 16 * (hwg ? j - 18 : 0);
  const int ob = lane >> 4, ocol = 2 * (lane & 7) + ((lane >> 3) & 1);
  u64 e_raw; v4u xr; unsigned wqv[16], w1v[16];
  int wA = w, wB = w, wC = w;
  asm volatile("" : "+s"(wA)); asm volatile("" : "+s"(wB)); asm volatile("" : "+s"(wC));
  WG_BARRIER();
  if (wA < 4) {
    e_raw = gload(ws + G_EPOCH);
    xr = *(const GAS v4u*)(p.x + w * DM + 8 * lane);
    const __amdgpu_buffer_rsrc_t rq = __builtin_amdgcn_make_buffer_rsrc((void*)(p.wq + g * 512 + 16 * j), 0, 512 * 8192 - 8192 + 32, RSRC_W3);
#pragma unroll
    for (int r = 0; r < 16; ++r) wqv[r] = __builtin_amdgcn_raw_buffer_load_b32(rq, rg * (16 * 8192) + cp * 4, r * 8192, AUX_W);
    const __amdgpu_buffer_rsrc_t r1 = __builtin_amdgcn_make_buffer_rsrc((void*)(p.w1 + f0), 0, hwg ? 511 * FF * 2 + 32 : 0, RSRC_W3);
#pragma unroll
    for (int r = 0; r < 16; ++r) w1v[r] = __builtin_amdgcn_raw_buffer_load_b32(r1, rg * (16 * FF * 2) + cp * 4, r * FF * 2, AUX_W);
    SCHED();
  }
  if (wC < 4) {
    const unsigned b1v = p.b1[f0 + ocol];
    SCHED();
    float* xs = (float*)(lds + L_XS);
    float* ys = (float*)(lds + L_YS);
    float* red = (float*)(lds + L_RED);
    float* redh = (float*)(lds + L_REDH);
    float xv[8], xn[8];
#pragma unroll
    for (int i = 0; i < 4; ++i) { xv[2 * i] = bflo(xr[i]); xv[2 * i + 1] = bfhi(xr[i]); }
    ln_row(xv, xn);
    *(f32x4*)(xs + w * DM + 8 * lane) = f32x4{xn[0], xn[1], xn[2], xn[3]};
    *(f32x4*)(xs + w * DM + 8 * lane + 4) = f32x4{xn[4], xn[5], xn[6], xn[7]};
    const u64 e_ = ((u64)__builtin_amdgcn_readfirstlane((unsigned)(e_raw >> 32)) << 32) | (u64)__builtin_amdgcn_readfirstlane((unsigned)e_raw);
    const u64 tag40 = ((e_ >> 24) * 0x5DEECE66DULL + 0xBULL) & ((1ULL << 40) - 1);
    Tag tag; tag.lo8 = (unsigned)(tag40 & 0xff) << 24; tag.hi = (unsigned)(tag40 >> 8);
    lds_set(lb + L_FA + 4 * w, ltag);
    STAMP(1);
    lds_wait4(lb + L_FA, ltag);
    red[w * 64 + lane] = gemv16(wqv, xs, rg, lane);
    lds_set(lb + L_FB + 4 * w, ltag);
    const __amdgpu_buffer_rsrc_t rqx = __builtin_amdgcn_make_buffer_rsrc((void*)(ws + G_QX + kbt * 4096 + kg * 512), 0, 4096, RSRC_W3);
    v4u gq[4];
    if (w == 0) {
      lds_wait4(lb + L_FB, ltag);
      const float q = (red[lane] + red[64 + lane]) + (red[128 + lane] + red[192 + lane]);
      gstore(ws + G_QX + ob * 4096 + g * 512 + 16 * j + ocol, f2bf(q), tag);
      STAMP(2);
    }
    if (hwg) {
      float y[8];
      ln_row(xn, y);
      *(f32x4*)(ys + w * DM + 8 * lane) = f32x4{y[0], y[1], y[2], y[3]};
      *(f32x4*)(ys + w * DM + 8 * lane + 4) = f32x4{y[4], y[5], y[6], y[7]};
      lds_set(lb + L_FC + 4 * w, ltag);
      lds_wait4(lb + L_FC, ltag);
      redh[w * 64 + lane] = gemv16(w1v, ys, rg, lane);
      lds_set(lb + L_FD + 4 * w, ltag);
      if (w == 1) {
        lds_wait4(lb + L_FD, ltag);
        const float hp = (redh[lane] + redh[64 + lane]) + (redh[128 + lane] + redh[192 + lane]);
        const float pre = rbf(rbf(hp) + bflo(b1v));
        const float hv = 0.5f * pre * (1.0f + tanhf(0.7978845608028654f * (pre + 0.044715f * pre * pre * pre)));
        gstore(ws + G_HX + ob * 2048 + f0 + ocol, f2bf(hv), tag);
      }
    }
    if (w == 0) {
      poll_pairs<4>(rqx, lane * 16, 1024, 0u, tag, gq);
#pragma unroll
      for (int k = 0; k < 4; ++k) ((unsigned*)(lds + L_QS))[64 * k + lane] = (gq[k].x & 0xffffu) | (gq[k].z << 16);
      *(v2u*)(lds + L_TAG) = v2u{tag.lo8, tag.hi};
      lds_set(lb + L_FQ, ltag);
      STAMP(3);
    }
  }
  SCHED();
  ISSUE_WINDOW(true);
  lds_wait1(lb + L_FQ, ltag);
  Tag tag;
  { const v2u t = *(const v2u*)(lds + L_TAG); tag.lo8 = __builtin_amdgcn_readfirstlane(t.x); tag.hi = __builtin_amdgcn_readfirstlane(t.y); }
  STAMP(4);
  const v4u qf = *(const v4u*)(lds + L_QS + ((lane & 15) >> 2) * 256 + ((lane & 3) + 4 * (lane >> 4)) * 16);

  float m = -INFINITY, lsum = 0.f;
  f32x4 acc[8];
#pragma unroll
  for (int i = 0; i < 8; ++i) acc[i] = f32x4{0.f, 0.f, 0.f, 0.f};
  float sA[8], sB[8];
#pragma nounroll
  for (int t = 0; t < nmine; t += 2) {
    const bool na = t + 2 < nmine, nb = t + 3 < nmine;
    if (w >= 4 && t + 2 * LATESIDE >= nmine) lds_set(lb + L_FL + 4 * (w - 4), ltag);
    const size_t oa = TOFF(na ? t + 2 : 0), ob2 = TOFF(nb ? t + 3 : 0);
    qk_tile(A.k, qf, sA, kb + oa, na, koff, 0.f);
    pv_tile(A.v, sA, m, lsum, acc, lane, vb + oa, na, voff, A.k, kb + oa, koff);
    qk_tile(B.k, qf, sB, kb + ob2, nb, koff, t + 1 < nmine ? 0.f : -INFINITY);
    pv_tile(B.v, sB, m, lsum, acc, lane, vb + ob2, nb, voff, B.k, kb + ob2, koff);
  }
  lsum = sum_rows(lsum);
  STAMP(5);
  float* om = (float*)(lds + L_OM);
  float* sm = (float*)(lds + L_SM);
  float* sl = (float*)(lds + L_SL);
  *(f32x4*)(om + w * 512 + lane * 8) = f32x4{acc[0][0], acc[1][0], acc[2][0], acc[3][0]};
  *(f32x4*)(om + w * 512 + lane * 8 + 4) = f32x4{acc[4][0], acc[5][0], acc[6][0], acc[7][0]};
  if (lane < 16 && (lane & 3) == 0) { sm[(lane >> 2) * 8 + w] = m; sl[(lane >> 2) * 8 + w] = lsum; }
  const int r8 = lane >> 3, c8 = lane & 7;

  v4u bw[2][8];
  if (w < 4) {
    lds_wait4(lb + L_FL, ltag);
    v4u t16[16];
    const __amdgpu_buffer_rsrc_t rwo = __builtin_amdgcn_make_buffer_rsrc((void*)(p.wo + (size_t)(g * 512 + 64 * s) * DM + 128 * w), 0, ((ABL_RED & 1) || ((ABL_RED & 8) && w != b)) ? 0 : 63 * 1024 + 256, RSRC_W3);
#pragma unroll
    for (int r = 0; r < 16; ++r) t16[r] = __builtin_amdgcn_raw_buffer_load_b128(rwo, (lane >> 4) * 1024 + (lane & 15) * 16, r * 4096, AUX_W2);
    v4u w2v[8];
    const int f0 = 256 * g + 64 * w;
    const __amdgpu_buffer_rsrc_t rw2 = __builtin_amdgcn_make_buffer_rsrc((void*)(p.w2 + (size_t)(g < 7 ? f0 : 0) * DM + 64 * s), 0, (g < 7 && !(ABL_RED & 2) && !((ABL_RED & 16) && w != b)) ? 63 * 1024 + 128 : 0, RSRC_W3);
#pragma unroll
    for (int i = 0; i < 8; ++i) w2v[i] = __builtin_amdgcn_raw_buffer_load_b128(rw2, r8 * 1024 + c8 * 16, i * 8192, AUX_W2);
    const u64* hsrc = ws + G_HX + b * 2048 + f0 + lane;
    u64 hg = gload(hsrc);
    SCHED();
#pragma unroll
    for (int ks = 0; ks < 2; ++ks)
#pragma unroll
      for (int jj = 0; jj < 4; ++jj)
#pragma unroll
        for (int k2 = 0; k2 < 4; ++k2) {
          bw[ks][2 * jj][k2] = __builtin_amdgcn_perm(t16[8 * ks + 2 * k2 + 1][jj], t16[8 * ks + 2 * k2][jj], 0x05040100u);
          bw[ks][2 * jj + 1][k2] = __builtin_amdgcn_perm(t16[8 * ks + 2 * k2 + 1][jj], t16[8 * ks + 2 * k2][jj], 0x07060302u);
        }
    if (g < 7 && !(ABL_RED & 4)) {
      if (!__all(gok(hg, tag))) {
        unsigned spin = 0;
        do { __builtin_amdgcn_s_sleep(PSLEEP); hg = gload(hsrc); } while (!__all(gok(hg, tag)) && ++spin < SPIN_CAP);
      }
      const float hl = bflo((unsigned)hg);
      float ma[8];
#pragma unroll
      for (int e = 0; e < 8; ++e) ma[e] = 0.f;
#pragma unroll
      for (int i = 0; i < 8; ++i) {
        const float hv = __shfl(hl, 8 * i + r8);
#pragma unroll
        for (int e = 0; e < 4; ++e) { ma[2 * e] = fmaf(hv, bflo(w2v[i][e]), ma[2 * e]); ma[2 * e + 1] = fmaf(hv, bfhi(w2v[i][e]), ma[2 * e + 1]); }
      }
      const float r = treduce8(ma, lane);
      gstore(ws + G_MX + ((size_t)(b * 64 + 8 * s + c8) * 32 + 4 * g + w) * 8 + r8, enc24(r), tag);
    }
  }
  STAMP(6);
  const unsigned xp = *(const GAS unsigned*)(p.x + b * DM + cb * 8 + 2 * (lane & 3));
  WG_BARRIER();
  {
    const int h = tid >> 7;
    const f32x4 m0 = *(const f32x4*)(sm + h * 8), m1 = *(const f32x4*)(sm + h * 8 + 4);
    float ov[8];
#pragma unroll
    for (int ww = 0; ww < 8; ++ww) ov[ww] = om[ww * 512 + tid];
    const f32x4 l0 = *(const f32x4*)(sl + h * 8), l1 = *(const f32x4*)(sl + h * 8 + 4);
    SCHED();
    const float M = fmaxf(fmaxf(fmaxf(m0[0], m0[1]), fmaxf(m0[2], m0[3])), fmaxf(fmaxf(m1[0], m1[1]), fmaxf(m1[2], m1[3])));
    float O = 0.f, L = 0.f;
#pragma unroll
    for (int ww = 0; ww < 4; ++ww) { const float f = __builtin_amdgcn_exp2f(m0[ww] - M); O = fmaf(f, ov[ww], O); L = fmaf(f, l0[ww], L); }
#pragma unroll
    for (int ww = 0; ww < 4; ++ww) { const float f = __builtin_amdgcn_exp2f(m1[ww] - M); O = fmaf(f, ov[4 + ww], O); L = fmaf(f, l1[ww], L); }
    gstore(ws + G_PX + (size_t)(kpair * 8 + ks) * 512 + tid, enc24(O), tag);
    const int d = tid & 127;
    if (d < 4) {
      const unsigned u = (d & 2) ? asu(L) : asu(M);
      gstore(ws + G_PS + (size_t)((kpair * 4 + h) * 8 + ks) * 4 + d, (d & 1) ? (u & 0xffffu) : (u >> 16), tag);
    }
  }
  STAMP(7);
  if (ABL_T & 2) {} else if (w == 4 || (DUP1 && w == 6)) {
    if (DUP1 && w == 6) __builtin_amdgcn_s_sleep(DUP1);
    const __amdgpu_buffer_rsrc_t rp = __builtin_amdgcn_make_buffer_rsrc((void*)(ws + G_PX + (size_t)pair * 4096 + 64 * s), 0, 8 * 4096, RSRC_W3);
    const __amdgpu_buffer_rsrc_t rs = __builtin_amdgcn_make_buffer_rsrc((void*)(ws + G_PS + (size_t)(pair * 4 + (s >> 1)) * 32), 0, 256, RSRC_W3);
    const int poff = (lane >> 5) * 4096 + (lane & 31) * 16;
    v4u gp[4]; v2u gs;
#pragma nounroll
    for (unsigned spin = 0; spin < SPIN_CAP; ++spin) {
      asm volatile("" ::: "memory");
      gs = __builtin_amdgcn_raw_buffer_load_b64(rs, (lane & 31) * 8, 0, AUX_SC1);
#pragma unroll
      for (int k = 0; k < 4; ++k) gp[k] = __builtin_amdgcn_raw_buffer_load_b128(rp, poff, k * 8192, AUX_SC1);
      const unsigned bad = gbad2(gp[0], tag) | gbad2(gp[1], tag) | gbad2(gp[2], tag) | gbad2(gp[3], tag) | (gs.y ^ tag.hi) | ((gs.x ^ tag.lo8) & 0xff000000u);
      if (__all(bad == 0u)) break;
      __builtin_amdgcn_s_sleep(PSLEEP);
    }
    STAMP(8);
    const unsigned sv = gs.x & 0xffffu;
    const float Mk = asf((asu(dpp<0x00>(asf(sv))) << 16) | asu(dpp<0x55>(asf(sv))));
    const float Lk = asf((asu(dpp<0xAA>(asf(sv))) << 16) | asu(dpp<0xFF>(asf(sv))));
    float M = fmaxf(Mk, dpp<ROR4>(Mk)); M = fmaxf(M, dpp<ROR8>(M));
    { auto t = __builtin_amdgcn_permlane16_swap(asu(M), asu(M), false, false); M = fmaxf(asf(t[0]), asf(t[1])); }
    const float fk = __builtin_amdgcn_exp2f(Mk - M);
    float L = fk * Lk; L += dpp<ROR4>(L); L += dpp<ROR8>(L); L = tr16(L, L);
    float o0 = 0.f, o1 = 0.f;
#pragma unroll
    for (int k = 0; k < 4; ++k) {
      const float fe = asf(__builtin_amdgcn_readlane(asu(fk), 8 * k)), fo = asf(__builtin_amdgcn_readlane(asu(fk), 8 * k + 4));
      const float f = (lane >> 5) ? fo : fe;
      o0 = fmaf(f, asf(gp[k].x << 8), o0); o1 = fmaf(f, asf(gp[k].z << 8), o1);
    }
    const float ot = tr32(o0, o1);
    const int e = 2 * (lane & 31) + (lane >> 5);
    ((u16*)(lds + L_OV))[(e & 32) + (e & 3) * 8 + ((e >> 2) & 7)] = (u16)f2bf(RCP ? ot * __builtin_amdgcn_rcpf(L) : ot / L);
    lds_set(lb + L_FO, ltag);
  } else if (w < 4) {
    const __amdgpu_buffer_rsrc_t rsx = __builtin_amdgcn_make_buffer_rsrc((void*)(ws + G_SX + ((size_t)(b * 64 + 16 * w) * 64 + cb) * 8), 0, 16 * 4096, RSRC_W3);
    lds_wait1(lb + L_FO, ltag);
    const v4u a0 = *(const v4u*)(lds + L_OV + (lane >> 4) * 16), a1 = *(const v4u*)(lds + L_OV + 64 + (lane >> 4) * 16);
    f32x4 d[8];
#pragma unroll
    for (int jx = 0; jx < 8; ++jx) d[jx] = mfma(a0, bw[0][jx], f32x4{0.f, 0.f, 0.f, 0.f});
#pragma unroll
    for (int jx = 0; jx < 8; ++jx) d[jx] = mfma(a1, bw[1][jx], d[jx]);
    const int g4 = lane >> 4;
    const float v0 = (g4 & 2) ? ((g4 & 1) ? d[6][0] : d[4][0]) : ((g4 & 1) ? d[2][0] : d[0][0]);
    const float v1 = (g4 & 2) ? ((g4 & 1) ? d[7][0] : d[5][0]) : ((g4 & 1) ? d[3][0] : d[1][0]);
    __builtin_amdgcn_raw_buffer_store_b128(v4u{enc24(v0) | tag.lo8, tag.hi, enc24(v1) | tag.lo8, tag.hi}, rsx, (lane & 15) * 4096 + g4 * 16, 0, AUX_SC1);
    STAMP(9);
  } else if (w == 5 && !(ABL_T & 1)) {
    const unsigned b2p = *(const GAS unsigned*)(p.b2 + cb * 8 + 2 * (lane & 3));
    float m0, m1;
    gather8<2>(ws + G_MX + (size_t)(b * 64 + cb) * 256, 28, tag, lane, m0, m1);
    m0 = rbf(rbf(m0) + bflo(b2p)); m1 = rbf(rbf(m1) + bfhi(b2p));
    float2 gt;
    gt.x = rbf(m0 / (1.0f + __expf(-m0))); gt.y = rbf(m1 / (1.0f + __expf(-m1)));
    float s0, s1;
    gather8<4>(ws + G_SX + (size_t)(b * 64 + cb) * 512, 64, tag, lane, s0, s1);
    STAMP(10);
    const float o0 = bflo(xp) + rbf(rbf(s0) * gt.x), o1 = bfhi(xp) + rbf(rbf(s1) * gt.y);
    if (lane < 4) *(GAS unsigned*)(p.out + b * DM + cb * 8 + 2 * lane) = cvtpk(o0, o1);
    if (bid == 0 && lane == 0) __hip_atomic_store((GAS u64*)(ws + G_EPOCH), ((u64)tag.hi << 32) | tag.lo8, RLX_AGENT);
  }
}

}

extern "C" void kernel_launch(void* const* d_in, const int* in_sizes, int n_in,
                              void* d_out, int out_size, void* d_ws, size_t ws_size,
                              hipStream_t stream) {
  Params p;
  p.x = (const u16*)d_in[0]; p.k = (const u16*)d_in[1]; p.v = (const u16*)d_in[2];
  p.wq = (const u16*)d_in[3]; p.wo = (const u16*)d_in[4];
  p.w1 = (const u16*)d_in[6]; p.b1 = (const u16*)d_in[7]; p.w2 = (const u16*)d_in[8]; p.b2 = (const u16*)d_in[9];
  p.out = (u16*)d_out; p.ws = (u64*)d_ws;
  hipLaunchKernelGGL(fused_decode, dim3(NWG), dim3(NTHR), 0, stream, p);
}
```

```cpp
#include <hip/hip_runtime.h>
#include <hip/hip_bf16.h>
#include <stdint.h>

namespace {

typedef unsigned v4u __attribute__((ext_vector_type(4)));
typedef unsigned v2u __attribute__((ext_vector_type(2)));
typedef float f32x4 __attribute__((ext_vector_type(4)));
typedef short bf16x8 __attribute__((ext_vector_type(8)));
typedef unsigned long long u64;
typedef unsigned short u16;
#define GAS __attribute__((address_space(1)))
#define RLX_AGENT __ATOMIC_RELAXED, __HIP_MEMORY_SCOPE_AGENT

constexpr int DM = 512, SK = 16384, FF = 1792;
constexpr int NWG = 256, NTHR = 512;
constexpr int RSRC_W3 = 0x00020000;
constexpr int AUX_NT = 2, AUX_SC1 = 16;
constexpr unsigned SPIN_CAP = 1u << 16;

constexpr size_t G_EPOCH = 0;
constexpr size_t G_QX = 512;
constexpr size_t G_PS = G_QX + 4 * 4096;
constexpr size_t G_PX = G_PS + 32 * 8 * 16;
constexpr size_t G_SX = G_PX + 32 * 8 * 512;
constexpr size_t G_HX = G_SX + 4 * 64 * 64 * 8;
constexpr size_t G_MX = G_HX + 4 * 2048;

constexpr int L_OM = 0;
constexpr int L_XS = 16384, L_YS = L_XS + 8192;
constexpr int L_RED = 32768, L_REDH = L_RED + 1024, L_QS = L_REDH + 1024, L_SM = L_QS + 1024, L_SL = L_SM + 128;
constexpr int L_FA = L_SL + 128, L_FB = L_FA + 16, L_FC = L_FB + 16, L_FD = L_FC + 16, L_FQ = L_FD + 16, L_TAG = L_FQ + 16,
              L_FO = L_TAG + 16, L_FL = L_FO + 16;
constexpr int L_OV = L_FA + 128, LDS_BYTES = L_OV + 128;
static_assert(L_FA % 16 == 0 && L_OV % 16 == 0 && L_FL + 16 == L_OV, "lds");

struct Params {
  const u16* x; const u16* k; const u16* v; const u16* wq; const u16* wo;
  const u16* w1; const u16* b1; const u16* w2; const u16* b2;
  u16* out; u64* ws;
};

__device__ __forceinline__ float asf(unsigned u) { return __builtin_bit_cast(float, u); }
__device__ __forceinline__ unsigned asu(float f) { return __builtin_bit_cast(unsigned, f); }
__device__ __forceinline__ float bflo(unsigned u) { return asf(u << 16); }
__device__ __forceinline__ float bfhi(unsigned u) { return asf(u & 0xffff0000u); }
__device__ __forceinline__ unsigned f2bf(float f) { return (unsigned)__builtin_bit_cast(u16, (__bf16)f); }
__device__ __forceinline__ float rbf(float f) { return (float)(__bf16)f; }
__device__ __forceinline__ unsigned cvtpk(float lo, float hi) {
  typedef __bf16 bf2 __attribute__((ext_vector_type(2)));
  bf2 r; r.x = (__bf16)lo; r.y = (__bf16)hi;
  return __builtin_bit_cast(unsigned, r);
}
template <int CTRL> __device__ __forceinline__ float dpp(float x) {
  return __builtin_bit_cast(float, __builtin_amdgcn_mov_dpp(__builtin_bit_cast(int, x), CTRL, 0xf, 0xf, true));
}
constexpr int XOR1 = 0xB1, XOR2 = 0x4E, HMIR = 0x141, MIR = 0x140, ROR8 = 0x128, ROR4 = 0x124;
__device__ __forceinline__ float tr32(float a, float b) {
  auto r = __builtin_amdgcn_permlane32_swap(asu(a), asu(b), false, false);
  return asf(r[0]) + asf(r[1]);
}
__device__ __forceinline__ float tr16(float a, float b) {
  auto r = __builtin_amdgcn_permlane16_swap(asu(a), asu(b), false, false);
  return asf(r[0]) + asf(r[1]);
}
__device__ __forceinline__ float max_rows(float x) {
  auto s = __builtin_amdgcn_permlane16_swap(asu(x), asu(x), false, false);
  x = fmaxf(asf(s[0]), asf(s[1]));
  auto t = __builtin_amdgcn_permlane32_swap(asu(x), asu(x), false, false);
  return fmaxf(asf(t[0]), asf(t[1]));
}
__device__ __forceinline__ float sum_rows(float x) { x = tr16(x, x); return tr32(x, x); }
__device__ __forceinline__ float wave_sum(float x) {
  x += dpp<XOR1>(x); x += dpp<XOR2>(x); x += dpp<HMIR>(x); x += dpp<MIR>(x);
  return sum_rows(x);
}
__device__ __forceinline__ float treduce8(const float (&v)[8], int lane) {
  const float u0 = tr32(v[0], v[4]), u1 = tr32(v[1], v[5]), u2 = tr32(v[2], v[6]), u3 = tr32(v[3], v[7]);
  const float w0 = tr16(u0, u2), w1 = tr16(u1, u3);
  const float t0 = w0 + dpp<ROR8>(w0), t1 = w1 + dpp<ROR8>(w1);
  return (lane & 8) ? t1 : t0;
}
__device__ __forceinline__ void ln_row(const float (&v)[8], float (&o)[8]) {
  float s = 0.f;
#pragma unroll
  for (int i = 0; i < 8; ++i) s += v[i];
  s = wave_sum(s);
  const float mean = s * (1.0f / DM);
  float ss = 0.f;
#pragma unroll
  for (int i = 0; i < 8; ++i) { const float d = v[i] - mean; ss = fmaf(d, d, ss); }
  ss = wave_sum(ss);
  const float var_b = rbf(ss * (1.0f / DM)), m_b = rbf(mean);
  const float den = rbf(sqrtf(rbf(var_b + 1e-5f)));
#pragma unroll
  for (int i = 0; i < 8; ++i) o[i] = rbf(rbf(v[i] - m_b) / den);
}
#define WG_BARRIER() asm volatile("s_waitcnt lgkmcnt(0)\n\ts_barrier" ::: "memory")
#define SCHED() __builtin_amdgcn_sched_barrier(0)

struct Tag { unsigned lo8, hi; };
__device__ __forceinline__ void gstore(u64* p, unsigned payload24, Tag t) {
  __hip_atomic_store((GAS u64*)p, ((u64)t.hi << 32) | (u64)(payload24 | t.lo8), RLX_AGENT);
}
__device__ __forceinline__ u64 gload(const u64* p) { return __hip_atomic_load((const GAS u64*)p, RLX_AGENT); }
__device__ __forceinline__ bool gok(u64 g, Tag t) {
  return (unsigned)(g >> 32) == t.hi && ((unsigned)g & 0xff000000u) == t.lo8;
}
__device__ __forceinline__ unsigned gbad2(v4u g, Tag t) {
  return (((g.x ^ t.lo8) | (g.z ^ t.lo8)) & 0xff000000u) | (g.y ^ t.hi) | (g.w ^ t.hi);
}
__device__ __forceinline__ unsigned enc24(float f) { unsigned u = asu(f); u += 0x7fu + ((u >> 8) & 1u); return u >> 8; }

__device__ __forceinline__ f32x4 mfma(v4u a, v4u b, f32x4 c) {
  return __builtin_amdgcn_mfma_f32_16x16x32_bf16(__builtin_bit_cast(bf16x8, a), __builtin_bit_cast(bf16x8, b), c, 0, 0, 0);
}

__device__ __forceinline__ float gemv16(const unsigned (&wv)[16], const float* vec, int rg, int lane) {
  float acc[8];
#pragma unroll
  for (int bb = 0; bb < 4; ++bb) {
    float a0 = 0.f, a1 = 0.f;
#pragma unroll
    for (int q4 = 0; q4 < 4; ++q4) {
      const f32x4 x0 = *(const f32x4*)(vec + bb * DM + 16 * rg + 4 * q4);
#pragma unroll
      for (int r = 0; r < 4; ++r) { a0 = fmaf(x0[r], bflo(wv[4 * q4 + r]), a0); a1 = fmaf(x0[r], bfhi(wv[4 * q4 + r]), a1); }
    }
    acc[bb * 2] = a0; acc[bb * 2 + 1] = a1;
  }
  return treduce8(acc, lane);
}
__device__ __forceinline__ void lds_set(unsigned addr) {
  const unsigned one = 1u;
  asm volatile("s_waitcnt lgkmcnt(0)\n\tds_write_b32 %0, %1" :: "v"(addr), "v"(one) : "memory");
}
__device__ __forceinline__ void lds_wait4(unsigned addr) {
#pragma nounroll
  for (unsigned spin = 0; spin < (1u << 22); ++spin) {
    v4u f;
    asm volatile("ds_read_b128 %0, %1\n\ts_waitcnt lgkmcnt(0)" : "=v"(f) : "v"(addr) : "memory");
    if (__builtin_amdgcn_readfirstlane(f.x & f.y & f.z & f.w)) break;
    __builtin_amdgcn_s_sleep(1);
  }
}
__device__ __forceinline__ void lds_wait1(unsigned addr) {
#pragma nounroll
  for (unsigned spin = 0; spin < (1u << 22); ++spin) {
    unsigned f;
    asm volatile("ds_read_b32 %0, %1\n\ts_waitcnt lgkmcnt(0)" : "=v"(f) : "v"(addr) : "memory");
    if (__builtin_amdgcn_readfirstlane(f)) break;
    __builtin_amdgcn_s_sleep(1);
  }
}

__device__ __forceinline__ void ld8(v4u (&d)[8], const u16* base, bool valid, int off) {
  const __amdgpu_buffer_rsrc_t r = __builtin_amdgcn_make_buffer_rsrc((void*)base, 0, valid ? 8192 : 0, RSRC_W3);
#pragma unroll
  for (int i = 0; i < 8; ++i) d[i] = __builtin_amdgcn_raw_buffer_load_b128(r, off + (i & 3) * 1024, (i >> 2) * 4096, AUX_NT);
  SCHED();
}
__device__ __forceinline__ void qk_tile(v4u (&k)[8], v4u qf, float (&sc)[8], const u16* kn, bool nvalid, int koff) {
  constexpr float C2 = 0.08838834764831845f * 1.4426950408889634f;
  f32x4 d[8];
#pragma unroll
  for (int i = 0; i < 8; ++i) d[i] = mfma(k[i], qf, f32x4{0.f, 0.f, 0.f, 0.f});
  SCHED();
  ld8(k, kn, nvalid, koff);
#pragma unroll
  for (int i = 0; i < 8; ++i) sc[i] = ((dpp<0x00>(d[i][0]) + dpp<0x55>(d[i][1])) + (dpp<0xAA>(d[i][2]) + dpp<0xFF>(d[i][3]))) * C2;
}
__device__ __forceinline__ void pv_tile(v4u (&v)[8], const float (&sc)[8], float& m, float& l, f32x4 (&acc)[8], int lane,
                                        const u16* vn, bool nvalid, int voff) {
  float tm = fmaxf(fmaxf(fmaxf(sc[0], sc[1]), fmaxf(sc[2], sc[3])), fmaxf(fmaxf(sc[4], sc[5]), fmaxf(sc[6], sc[7])));
  tm = max_rows(tm);
  if (__any(tm > m)) {
    const float mn = fmaxf(m, tm), al = __builtin_amdgcn_exp2f(m - mn);
    m = mn; l *= al;
    const float ah = __shfl(al, (lane >> 4) * 4);
#pragma unroll
    for (int j = 0; j < 8; ++j) acc[j] *= ah;
  }
  v4u pk;
#pragma unroll
  for (int k = 0; k < 4; ++k) {
    const float p0 = __builtin_amdgcn_exp2f(sc[2 * k] - m), p1 = __builtin_amdgcn_exp2f(sc[2 * k + 1] - m);
    l += p0 + p1; pk[k] = cvtpk(p0, p1);
  }
#pragma unroll
  for (int jj = 0; jj < 4; ++jj) {
    v4u lo, hi;
#pragma unroll
    for (int k2 = 0; k2 < 4; ++k2) {
      lo[k2] = __builtin_amdgcn_perm(v[2 * k2 + 1][jj], v[2 * k2][jj], 0x05040100u);
      hi[k2] = __builtin_amdgcn_perm(v[2 * k2 + 1][jj], v[2 * k2][jj], 0x07060302u);
    }
    acc[2 * jj] = mfma(pk, lo, acc[2 * jj]); acc[2 * jj + 1] = mfma(pk, hi, acc[2 * jj + 1]);
  }
  SCHED();
  ld8(v, vn, nvalid, voff);
}
template <int NK> __device__ __forceinline__ void poll_pairs(__amdgpu_buffer_rsrc_t r, int voff, int kstride, unsigned skip, Tag tag, v4u (&gv)[NK]) {
  int off[NK];
#pragma unroll
  for (int k = 0; k < NK; ++k) { off[k] = ((skip >> k) & 1u) ? 0x40000000 : voff; gv[k] = v4u{0u, 0u, 0u, 0u}; }
#pragma nounroll
  for (unsigned spin = 0; spin < SPIN_CAP; ++spin) {
    asm volatile("" ::: "memory");
    v4u t[NK];
#pragma unroll
    for (int k = 0; k < NK; ++k) t[k] = __builtin_amdgcn_raw_buffer_load_b128(r, off[k], k * kstride, AUX_SC1);
    bool pend = false;
#pragma unroll
    for (int k = 0; k < NK; ++k) {
      const bool got = gbad2(t[k], tag) == 0u, live = off[k] == voff;
      if (got & live) { gv[k].x = t[k].x; gv[k].z = t[k].z; off[k] = 0x40000000; }
      pend |= live & !got;
    }
    if (!__any(pend)) break;
    __builtin_amdgcn_s_sleep(2);
  }
}
template <int NK> __device__ __forceinline__ void gather8(const u64* src, int nparts, Tag tag, int lane, float& s0, float& s1) {
  const __amdgpu_buffer_rsrc_t r = __builtin_amdgcn_make_buffer_rsrc((void*)src, 0, NK * 1024, RSRC_W3);
  v4u gv[NK];
  unsigned skip = 0u;
#pragma unroll
  for (int k = 0; k < NK; ++k) skip |= (16 * k + (lane >> 2) >= nparts) ? (1u << k) : 0u;
  poll_pairs<NK>(r, lane * 16, 1024, skip, tag, gv);
  s0 = 0.f; s1 = 0.f;
#pragma unroll
  for (int k = 0; k < NK; ++k) { s0 += asf(gv[k].x << 8); s1 += asf(gv[k].z << 8); }
  s0 += dpp<ROR4>(s0); s1 += dpp<ROR4>(s1);
  s0 += dpp<ROR8>(s0); s1 += dpp<ROR8>(s1);
  s0 = sum_rows(s0); s1 = sum_rows(s1);
}

__global__ void __launch_bounds__(NTHR, 2) fused_decode(Params p) {
  __shared__ __attribute__((aligned(16))) unsigned char lds[LDS_BYTES];
  const int tid = threadIdx.x, lane = tid & 63, w = __builtin_amdgcn_readfirstlane(tid >> 6);
  const int bid = blockIdx.x, g = bid & 7, j = bid >> 3, b = j >> 3, s = j & 7, pair = b * 8 + g, cb = g * 8 + s;
  const int kbt = g >> 1, kg = j >> 2, ks = (j & 3) * 2 + (g & 1), kpair = kbt * 8 + kg;
  u64* ws = p.ws;
  const unsigned lb = (unsigned)(size_t)(__attribute__((address_space(3))) unsigned char*)lds;

  if (tid < 32) ((unsigned*)(lds + L_FA))[tid] = 0u;
  const size_t kvoff = ((size_t)kpair * SK + ks * 2048 + w * 32) * 128;
  const u16* kb = p.k + kvoff;
  const u16* vb = p.v + kvoff;
  const int koff = ((lane & 15) >> 2) * 256 + (lane & 3) * 16 + (lane >> 4) * 64, voff = lane * 16;
  WG_BARRIER();
  if (w < 4) {
    const u64 e_raw = gload(ws + G_EPOCH);
    const v4u xr = *(const GAS v4u*)(p.x + w * DM + 8 * lane);
    const int cp = tid & 7, rg = tid >> 3;
    const bool hwg = j >= 18;
    const int f0 = 224 * g + 16 * (hwg ? j - 18 : 0);
    const int ob = lane >> 4, ocol = 2 * (lane & 7) + ((lane >> 3) & 1);
    unsigned wqv[16], w1v[16];
    const __amdgpu_buffer_rsrc_t rq = __builtin_amdgcn_make_buffer_rsrc((void*)(p.wq + g * 512 + 16 * j), 0, 511 * 8192 + 32, RSRC_W3);
#pragma unroll
    for (int r = 0; r < 16; ++r) wqv[r] = __builtin_amdgcn_raw_buffer_load_b32(rq, rg * (16 * 8192) + cp * 4, r * 8192, 0);
    const __amdgpu_buffer_rsrc_t r1 = __builtin_amdgcn_make_buffer_rsrc((void*)(p.w1 + f0), 0, hwg ? 511 * FF * 2 + 32 : 0, RSRC_W3);
#pragma unroll
    for (int r = 0; r < 16; ++r) w1v[r] = __builtin_amdgcn_raw_buffer_load_b32(r1, rg * (16 * FF * 2) + cp * 4, r * FF * 2, 0);
    const unsigned b1v = p.b1[f0 + ocol];
    SCHED();
    float* xs = (float*)(lds + L_XS);
    float* ys = (float*)(lds + L_YS);
    float* red = (float*)(lds + L_RED);
    float* redh = (float*)(lds + L_REDH);
    float xv[8], xn[8];
#pragma unroll
    for (int i = 0; i < 4; ++i) { xv[2 * i] = bflo(xr[i]); xv[2 * i + 1] = bfhi(xr[i]); }
    ln_row(xv, xn);
    *(f32x4*)(xs + w * DM + 8 * lane) = f32x4{xn[0], xn[1], xn[2], xn[3]};
    *(f32x4*)(xs + w * DM + 8 * lane + 4) = f32x4{xn[4], xn[5], xn[6], xn[7]};
    const u64 e_ = ((u64)__builtin_amdgcn_readfirstlane((unsigned)(e_raw >> 32)) << 32) | (u64)__builtin_amdgcn_readfirstlane((unsigned)e_raw);
    const u64 tag40 = ((e_ >> 24) * 0x5DEECE66DULL + 0xBULL) & ((1ULL << 40) - 1);
    Tag tag; tag.lo8 = (unsigned)(tag40 & 0xff) << 24; tag.hi = (unsigned)(tag40 >> 8);
    lds_set(lb + L_FA + 4 * w);
    lds_wait4(lb + L_FA);
    red[w * 64 + lane] = gemv16(wqv, xs, rg, lane);
    lds_set(lb + L_FB + 4 * w);
    if (w == 0) {
      lds_wait4(lb + L_FB);
      const float q = (red[lane] + red[64 + lane]) + (red[128 + lane] + red[192 + lane]);
      gstore(ws + G_QX + ob * 4096 + g * 512 + 16 * j + ocol, f2bf(q), tag);
    }
    if (hwg) {
      float y[8];
      ln_row(xn, y);
      *(f32x4*)(ys + w * DM + 8 * lane) = f32x4{y[0], y[1], y[2], y[3]};
      *(f32x4*)(ys + w * DM + 8 * lane + 4) = f32x4{y[4], y[5], y[6], y[7]};
      lds_set(lb + L_FC + 4 * w);
      lds_wait4(lb + L_FC);
      redh[w * 64 + lane] = gemv16(w1v, ys, rg, lane);
      lds_set(lb + L_FD + 4 * w);
      if (w == 1) {
        lds_wait4(lb + L_FD);
        const float hp = (redh[lane] + redh[64 + lane]) + (redh[128 + lane] + redh[192 + lane]);
        const float pre = rbf(rbf(hp) + bflo(b1v));
        const float hv = 0.5f * pre * (1.0f + tanhf(0.7978845608028654f * (pre + 0.044715f * pre * pre * pre)));
        gstore(ws + G_HX + ob * 2048 + f0 + ocol, f2bf(hv), tag);
      }
    }
    if (w == 0) {
      const __amdgpu_buffer_rsrc_t rqx = __builtin_amdgcn_make_buffer_rsrc((void*)(ws + G_QX + kbt * 4096 + kg * 512), 0, 4096, RSRC_W3);
      v4u gq[4];
      poll_pairs<4>(rqx, lane * 16, 1024, 0u, tag, gq);
#pragma unroll
      for (int k = 0; k < 4; ++k) ((unsigned*)(lds + L_QS))[64 * k + lane] = (gq[k].x & 0xffffu) | (gq[k].z << 16);
      *(v2u*)(lds + L_TAG) = v2u{tag.lo8, tag.hi};
      lds_set(lb + L_FQ);
    }
  }
  SCHED();
  v4u kA[8], vA[8], kB[8], vB[8];
  ld8(kA, kb, true, koff); ld8(vA, vb, true, voff);
  ld8(kB, kb + 32768, true, koff); ld8(vB, vb + 32768, true, voff);
  lds_wait1(lb + L_FQ);
  Tag tag;
  { const v2u t = *(const v2u*)(lds + L_TAG); tag.lo8 = __builtin_amdgcn_readfirstlane(t.x); tag.hi = __builtin_amdgcn_readfirstlane(t.y); }
  const v4u qf = *(const v4u*)(lds + L_QS + ((lane & 15) >> 2) * 256 + ((lane & 3) + 4 * (lane >> 4)) * 16);

  float m = -INFINITY, lsum = 0.f;
  f32x4 acc[8];
#pragma unroll
  for (int i = 0; i < 8; ++i) acc[i] = f32x4{0.f, 0.f, 0.f, 0.f};
  float sA[8], sB[8];
#pragma nounroll
  for (int t = 0; t < 8; t += 2) {
    const bool nv = t < 6;
    if (w >= 4 && t >= 2) lds_set(lb + L_FL + 4 * (w - 4));
    const size_t o = (size_t)(nv ? t + 2 : 0) * 32768;
    qk_tile(kA, qf, sA, kb + o, nv, koff);
    pv_tile(vA, sA, m, lsum, acc, lane, vb + o, nv, voff);
    qk_tile(kB, qf, sB, kb + o + 32768, nv, koff);
    pv_tile(vB, sB, m, lsum, acc, lane, vb + o + 32768, nv, voff);
  }
  lsum = sum_rows(lsum);
  float* om = (float*)(lds + L_OM);
  float* sm = (float*)(lds + L_SM);
  float* sl = (float*)(lds + L_SL);
  *(f32x4*)(om + w * 512 + lane * 8) = f32x4{acc[0][0], acc[1][0], acc[2][0], acc[3][0]};
  *(f32x4*)(om + w * 512 + lane * 8 + 4) = f32x4{acc[4][0], acc[5][0], acc[6][0], acc[7][0]};
  if (lane < 16 && (lane & 3) == 0) { sm[(lane >> 2) * 8 + w] = m; sl[(lane >> 2) * 8 + w] = lsum; }

  v4u bw[2][8];
  if (w < 4) {
    const int r8 = lane >> 3, c8 = lane & 7;
    lds_wait4(lb + L_FL);
    v4u t16[16];
    const __amdgpu_buffer_rsrc_t rwo = __builtin_amdgcn_make_buffer_rsrc((void*)(p.wo + (size_t)(g * 512 + 64 * s) * DM + 128 * w), 0, 63 * 1024 + 256, RSRC_W3);
#pragma unroll
    for (int r = 0; r < 16; ++r) t16[r] = __builtin_amdgcn_raw_buffer_load_b128(rwo, (lane >> 4) * 1024 + (lane & 15) * 16, r * 4096, 0);
    v4u w2v[8];
    const int f0 = 256 * g + 64 * w;
    const __amdgpu_buffer_rsrc_t rw2 = __builtin_amdgcn_make_buffer_rsrc((void*)(p.w2 + (size_t)(g < 7 ? f0 : 0) * DM + 64 * s), 0, g < 7 ? 63 * 1024 + 128 : 0, RSRC_W3);
#pragma unroll
    for (int i = 0; i < 8; ++i) w2v[i] = __builtin_amdgcn_raw_buffer_load_b128(rw2, r8 * 1024 + c8 * 16, i * 8192, 0);
    const u64* hsrc = ws + G_HX + b * 2048 + f0 + lane;
    u64 hg = gload(hsrc);
    SCHED();
#pragma unroll
    for (int kk = 0; kk < 2; ++kk)
#pragma unroll
      for (int jj = 0; jj < 4; ++jj)
#pragma unroll
        for (int k2 = 0; k2 < 4; ++k2) {
          bw[kk][2 * jj][k2] = __builtin_amdgcn_perm(t16[8 * kk + 2 * k2 + 1][jj], t16[8 * kk + 2 * k2][jj], 0x05040100u);
          bw[kk][2 * jj + 1][k2] = __builtin_amdgcn_perm(t16[8 * kk + 2 * k2 + 1][jj], t16[8 * kk + 2 * k2][jj], 0x07060302u);
        }
    if (g < 7) {
      if (!__all(gok(hg, tag))) {
        unsigned spin = 0;
        do { __builtin_amdgcn_s_sleep(2); hg = gload(hsrc); } while (!__all(gok(hg, tag)) && ++spin < SPIN_CAP);
      }
      const float hl = bflo((unsigned)hg);
      float ma[8];
#pragma unroll
      for (int e = 0; e < 8; ++e) ma[e] = 0.f;
#pragma unroll
      for (int i = 0; i < 8; ++i) {
        const float hv = __shfl(hl, 8 * i + r8);
#pragma unroll
        for (int e = 0; e < 4; ++e) { ma[2 * e] = fmaf(hv, bflo(w2v[i][e]), ma[2 * e]); ma[2 * e + 1] = fmaf(hv, bfhi(w2v[i][e]), ma[2 * e + 1]); }
      }
      const float r = treduce8(ma, lane);
      gstore(ws + G_MX + ((size_t)(b * 64 + 8 * s + c8) * 32 + 4 * g + w) * 8 + r8, enc24(r), tag);
    }
  }

  WG_BARRIER();
  {
    const int h = tid >> 7;
    const f32x4 m0 = *(const f32x4*)(sm + h * 8), m1 = *(const f32x4*)(sm + h * 8 + 4);
    float ov[8];
#pragma unroll
    for (int ww = 0; ww < 8; ++ww) ov[ww] = om[ww * 512 + tid];
    const f32x4 l0 = *(const f32x4*)(sl + h * 8), l1 = *(const f32x4*)(sl + h * 8 + 4);
    SCHED();
    const float M = fmaxf(fmaxf(fmaxf(m0[0], m0[1]), fmaxf(m0[2], m0[3])), fmaxf(fmaxf(m1[0], m1[1]), fmaxf(m1[2], m1[3])));
    float O = 0.f, L = 0.f;
#pragma unroll
    for (int ww = 0; ww < 4; ++ww) { const float f = __builtin_amdgcn_exp2f(m0[ww] - M); O = fmaf(f, ov[ww], O); L = fmaf(f, l0[ww], L); }
#pragma unroll
    for (int ww = 0; ww < 4; ++ww) { const float f = __builtin_amdgcn_exp2f(m1[ww] - M); O = fmaf(f, ov[4 + ww], O); L = fmaf(f, l1[ww], L); }
    gstore(ws + G_PX + (size_t)(kpair * 8 + ks) * 512 + tid, enc24(O), tag);
    const int d = tid & 127;
    if (d < 4) {
      const unsigned u = (d & 2) ? asu(L) : asu(M);
      gstore(ws + G_PS + (size_t)((kpair * 4 + h) * 8 + ks) * 4 + d, (d & 1) ? (u & 0xffffu) : (u >> 16), tag);
    }
  }
  if (w == 4) {
    const __amdgpu_buffer_rsrc_t rp = __builtin_amdgcn_make_buffer_rsrc((void*)(ws + G_PX + (size_t)pair * 4096 + 64 * s), 0, 7 * 4096 + 512, RSRC_W3);
    const __amdgpu_buffer_rsrc_t rs = __builtin_amdgcn_make_buffer_rsrc((void*)(ws + G_PS + (size_t)(pair * 4 + (s >> 1)) * 32), 0, 256, RSRC_W3);
    const int poff = (lane >> 5) * 4096 + (lane & 31) * 16;
    v4u gp[4]; v2u gs;
#pragma nounroll
    for (unsigned spin = 0; spin < SPIN_CAP; ++spin) {
      asm volatile("" ::: "memory");
      gs = __builtin_amdgcn_raw_buffer_load_b64(rs, (lane & 31) * 8, 0, AUX_SC1);
#pragma unroll
      for (int k = 0; k < 4; ++k) gp[k] = __builtin_amdgcn_raw_buffer_load_b128(rp, poff, k * 8192, AUX_SC1);
      const unsigned bad = gbad2(gp[0], tag) | gbad2(gp[1], tag) | gbad2(gp[2], tag) | gbad2(gp[3], tag) | (gs.y ^ tag.hi) | ((gs.x ^ tag.lo8) & 0xff000000u);
      if (__all(bad == 0u)) break;
      __builtin_amdgcn_s_sleep(2);
    }
    const unsigned sv = gs.x & 0xffffu;
    const float Mk = asf((asu(dpp<0x00>(asf(sv))) << 16) | asu(dpp<0x55>(asf(sv))));
    const float Lk = asf((asu(dpp<0xAA>(asf(sv))) << 16) | asu(dpp<0xFF>(asf(sv))));
    float M = fmaxf(Mk, dpp<ROR4>(Mk)); M = fmaxf(M, dpp<ROR8>(M));
    { auto t = __builtin_amdgcn_permlane16_swap(asu(M), asu(M), false, false); M = fmaxf(asf(t[0]), asf(t[1])); }
    const float fk = __builtin_amdgcn_exp2f(Mk - M);
    float L = fk * Lk; L += dpp<ROR4>(L); L += dpp<ROR8>(L); L = tr16(L, L);
    float o0 = 0.f, o1 = 0.f;
#pragma unroll
    for (int k = 0; k < 4; ++k) {
      const float fe = asf(__builtin_amdgcn_readlane(asu(fk), 8 * k)), fo = asf(__builtin_amdgcn_readlane(asu(fk), 8 * k + 4));
      const float f = (lane >> 5) ? fo : fe;
      o0 = fmaf(f, asf(gp[k].x << 8), o0); o1 = fmaf(f, asf(gp[k].z << 8), o1);
    }
    const float ot = tr32(o0, o1);
    const int e = 2 * (lane & 31) + (lane >> 5);
    ((u16*)(lds + L_OV))[(e & 32) + (e & 3) * 8 + ((e >> 2) & 7)] = (u16)f2bf(ot / L);
    lds_set(lb + L_FO);
  } else if (w < 4) {
    const __amdgpu_buffer_rsrc_t rsx = __builtin_amdgcn_make_buffer_rsrc((void*)(ws + G_SX + ((size_t)(b * 64 + 16 * w) * 64 + cb) * 8), 0, 15 * 4096 + 64, RSRC_W3);
    lds_wait1(lb + L_FO);
    const v4u a0 = *(const v4u*)(lds + L_OV + (lane >> 4) * 16), a1 = *(const v4u*)(lds + L_OV + 64 + (lane >> 4) * 16);
    f32x4 d[8];
#pragma unroll
    for (int jx = 0; jx < 8; ++jx) d[jx] = mfma(a0, bw[0][jx], f32x4{0.f, 0.f, 0.f, 0.f});
#pragma unroll
    for (int jx = 0; jx < 8; ++jx) d[jx] = mfma(a1, bw[1][jx], d[jx]);
    const int g4 = lane >> 4;
    const float v0 = (g4 & 2) ? ((g4 & 1) ? d[6][0] : d[4][0]) : ((g4 & 1) ? d[2][0] : d[0][0]);
    const float v1 = (g4 & 2) ? ((g4 & 1) ? d[7][0] : d[5][0]) : ((g4 & 1) ? d[3][0] : d[1][0]);
    __builtin_amdgcn_raw_buffer_store_b128(v4u{enc24(v0) | tag.lo8, tag.hi, enc24(v1) | tag.lo8, tag.hi}, rsx, (lane & 15) * 4096 + g4 * 16, 0, AUX_SC1);
  } else if (w == 5) {
    const unsigned xp = *(const GAS unsigned*)(p.x + b * DM + cb * 8 + 2 * (lane & 3));
    const unsigned b2p = *(const GAS unsigned*)(p.b2 + cb * 8 + 2 * (lane & 3));
    float m0, m1;
    gather8<2>(ws + G_MX + (size_t)(b * 64 + cb) * 256, 28, tag, lane, m0, m1);
    m0 = rbf(rbf(m0) + bflo(b2p)); m1 = rbf(rbf(m1) + bfhi(b2p));
    const float g0 = rbf(m0 / (1.0f + __expf(-m0))), g1 = rbf(m1 / (1.0f + __expf(-m1)));
    float s0, s1;
    gather8<4>(ws + G_SX + (size_t)(b * 64 + cb) * 512, 64, tag, lane, s0, s1);
    const float o0 = bflo(xp) + rbf(rbf(s0) * g0), o1 = bfhi(xp) + rbf(rbf(s1) * g1);
    if (lane < 4) *(GAS unsigned*)(p.out + b * DM + cb * 8 + 2 * lane) = cvtpk(o0, o1);
    if (bid == 0 && lane == 0) __hip_atomic_store((GAS u64*)(ws + G_EPOCH), ((u64)tag.hi << 32) | tag.lo8, RLX_AGENT);
  }
}

}

extern "C" void kernel_launch(void* const* d_in, const int* in_sizes, int n_in,
                              void* d_out, int out_size, void* d_ws, size_t ws_size,
                              hipStream_t stream) {
  Params p;
  p.x = (const u16*)d_in[0]; p.k = (const u16*)d_in[1]; p.v = (const u16*)d_in[2];
  p.wq = (const u16*)d_in[3]; p.wo = (const u16*)d_in[4];
  p.w1 = (const u16*)d_in[6]; p.b1 = (const u16*)d_in[7]; p.w2 = (const u16*)d_in[8]; p.b2 = (const u16*)d_in[9];
  p.out = (u16*)d_out; p.ws = (u64*)d_ws;
  hipLaunchKernelGGL(fused_decode, dim3(NWG), dim3(NTHR), 0, stream, p);
}
```
